```python
import torch

def _softmax(x, axis=-1):
    return torch.softmax(x, dim=axis)
def _gelu(x):
    return torch.nn.functional.gelu(x, approximate="tanh")
def _silu(x):
    return torch.nn.functional.silu(x)
def _sigmoid(x):
    return torch.sigmoid(x)
def _layernorm(x, axis=-1, eps=1e-5):
    m = x.mean(dim=axis, keepdim=True)
    v = x.var(dim=axis, keepdim=True, unbiased=False)
    return (x - m) / torch.sqrt(v + eps)
def _scatter_add(tgt, idx, src, axis=0):
    return tgt.clone().index_add_(axis, idx, src)
def _segment_sum(src, seg, n):
    out = torch.zeros((n,) + src.shape[1:], dtype=src.dtype)
    return out.index_add_(0, seg, src)
def _pad(x, pw):
    flat = tuple(v for pair in reversed(pw) for v in pair)
    return torch.nn.functional.pad(x, flat)

kernel_name = 'gated_mix__mha_block__moe_arch_mod__prompt'

def setup_inputs():

    _w = (lambda a: (lambda t: t.to(torch.bfloat16) if t.is_floating_point() else t)(torch.as_tensor(a)))
    def _A_setup():
        B, S, D, H = 1, 2048, 512, 32
        return {
            "x": _w((lambda *s: torch.randn(*s, dtype=torch.bfloat16) * 0.05)(B, S, D)),
            "wqkv": _w((lambda *s: torch.randn(*s, dtype=torch.bfloat16) * 0.8)(D, 3 * D)),
            "wo": _w((lambda *s: torch.randn(*s, dtype=torch.bfloat16) * 0.1)(D, D)),
        }
    _A_in = _A_setup()

    def _B_setup():
        B, S, D, F, C = 1, 2048, 512, 1792, 1
        return {
            "x": _w((lambda *s: torch.randn(*s, dtype=torch.bfloat16) * 0.05)(B, S, D)),
            "w_r": _w((lambda *s: torch.randn(*s, dtype=torch.bfloat16) * 1.6)(D)),
            "w_gate": _w((lambda *s: torch.randn(*s, dtype=torch.bfloat16) * 0.2)(D, F)),
            "w_up": _w((lambda *s: torch.randn(*s, dtype=torch.bfloat16) * 0.2)(D, F)),
            "w_down": _w((lambda *s: torch.randn(*s, dtype=torch.bfloat16) * 0.2)(F, D)),
        }
    _B_in = _B_setup()

    return {
        "A_x": _A_in["x"],
        "A_wqkv": _A_in["wqkv"],
        "A_wo": _A_in["wo"],
        "B_x": _B_in["x"],
        "B_w_r": _B_in["w_r"],
        "B_w_gate": _B_in["w_gate"],
        "B_w_up": _B_in["w_up"],
        "B_w_down": _B_in["w_down"],
        "gate_w": _w((lambda *s: torch.randn(*s, dtype=torch.bfloat16) * 0.05)(512) * 16.0),
    }

def reference(A_x, A_wqkv, A_wo, B_x, B_w_r, B_w_gate, B_w_up, B_w_down, gate_w):
    def _A_ref(x, wqkv, wo):
        B, S, D = x.shape
        H = 32
        Dh = D // H
        qkv = (lambda x, *d: x.permute(*d))((x @ wqkv).reshape(B, S, 3, H, Dh), 2, 0, 3, 1, 4)
        q, k, v = qkv[0], qkv[1], qkv[2]
        scores = torch.einsum("bhqd,bhkd->bhqk", q, k) * (Dh ** -0.5)
        p = _softmax(scores, axis=-1)
        o = torch.einsum("bhqk,bhkd->bhqd", p, v)
        return (lambda x, *d: x.permute(*d))(o, 0, 2, 1, 3).reshape(B, S, D) @ wo

    def _B_ref(x, w_r, w_gate, w_up, w_down):
        B, S, D = x.shape
        C = 1
        r = x @ w_r
        idx = torch.argsort(-r, axis=-1)[:, :C]
        idx_e = (lambda x, n, axis: torch.repeat_interleave(x, n, dim=axis))(idx.reshape(B, C, 1), D, axis=2)
        x_sel = (lambda x, i, axis: torch.take_along_dim(x, i, dim=axis))(x, idx_e, axis=1)
        g = _silu(x_sel @ w_gate)
        u = x_sel @ w_up
        y = (g * u) @ w_down
        r_sel = _sigmoid((lambda x, i, axis: torch.take_along_dim(x, i, dim=axis))(r, idx, axis=1))
        y = y * r_sel.reshape(B, C, 1)
        b_off = (torch.arange(B) * S).reshape(B, 1)
        flat_idx = (idx + b_off).reshape(B * C)
        zero = (x * 0.0).reshape(B * S, D)
        upd = _scatter_add(zero, flat_idx, y.reshape(B * C, D), axis=0)
        return x + upd.reshape(B, S, D)

    x = A_x
    g = _sigmoid((x * gate_w).sum(-1, keepdims=True))
    a = _A_ref(x, A_wqkv, A_wo)
    b = _B_ref(x, B_w_r, B_w_gate, B_w_up, B_w_down)
    return g * a + (1 - g) * b
```

```cpp
#include <hip/hip_runtime.h>
#include <hip/hip_bf16.h>

typedef unsigned short u16;
typedef unsigned int u32;
typedef unsigned long long u64;
typedef __attribute__((ext_vector_type(8))) __bf16 bf16x8;
typedef __attribute__((ext_vector_type(16))) float f32x16;
typedef __attribute__((ext_vector_type(4))) float f32x4;
typedef __attribute__((ext_vector_type(4))) unsigned u32x4;
typedef __attribute__((ext_vector_type(2))) unsigned u32x2;
typedef __attribute__((ext_vector_type(4))) short s16x4;
#define LAS __attribute__((address_space(3)))

constexpr int S_ = 2048, D_ = 512, F_ = 1792;
constexpr size_t WS_KV = 0;
constexpr size_t WS_OF = 4u << 20;
constexpr size_t WS_QF = 6u << 20;
constexpr size_t WS_G  = 8u << 20;
constexpr size_t WS_R  = WS_G + 8192;
constexpr size_t WS_H  = WS_R + 8192;
constexpr size_t WS_Y  = WS_H + 8192;
constexpr size_t WS_END = WS_Y + 4096;

__device__ __forceinline__ void glds16(const void* g, unsigned lds_base) {
  unsigned sv;
  asm volatile("s_mov_b32 %0, m0\n\ts_mov_b32 m0, %2\n\ts_nop 0\n\tglobal_load_lds_dwordx4 %1, off\n\ts_mov_b32 m0, %0"
               : "=&s"(sv) : "v"(g), "s"(lds_base) : "memory");
}
__device__ __forceinline__ unsigned cvtpk(float lo, float hi) {
  typedef __attribute__((ext_vector_type(2))) __bf16 bf16x2;
  typedef __attribute__((ext_vector_type(2))) float f32x2;
  f32x2 v = {lo, hi};
  return __builtin_bit_cast(unsigned, __builtin_convertvector(v, bf16x2));
}
__device__ __forceinline__ float bfround(float f) { return __uint_as_float(cvtpk(f, 0.f) << 16); }
__device__ __forceinline__ float bflo(unsigned u) { return __uint_as_float(u << 16); }
__device__ __forceinline__ float bfhi(unsigned u) { return __uint_as_float(u & 0xffff0000u); }
__device__ __forceinline__ float sigmoidf_(float z) { return 1.f / (1.f + __expf(-z)); }
__device__ __forceinline__ bf16x8 lds_b128(unsigned a) { return *(const LAS bf16x8*)(size_t)a; }
__device__ __forceinline__ s16x4 lds_tr(unsigned a) {
  return __builtin_amdgcn_ds_read_tr16_b64_v4i16((LAS s16x4*)(size_t)a);
}
__device__ __forceinline__ bf16x8 cat8(s16x4 a, s16x4 b) {
  typedef __attribute__((ext_vector_type(8))) short s16x8;
  s16x8 r = {a[0], a[1], a[2], a[3], b[0], b[1], b[2], b[3]};
  return __builtin_bit_cast(bf16x8, r);
}
#define MFMA32(a, b, c) __builtin_amdgcn_mfma_f32_32x32x16_bf16(a, b, c, 0, 0, 0)
#define MFMA16(a, b, c) __builtin_amdgcn_mfma_f32_16x16x32_bf16(a, b, c, 0, 0, 0)
#define WAIT_BAR(N) asm volatile("s_waitcnt vmcnt(" #N ") lgkmcnt(0)\n\ts_barrier" ::: "memory")

__device__ __forceinline__ void decode_block(int b, int& hg, int& rb) {
  const int xcd = b & 7, j = b >> 3;
  hg = 2 * (xcd & 3) + (j & 1);
  rb = 16 * (xcd >> 2) + (j >> 1);
}

constexpr int L_X = 0, L_W = 65536, L_WV = 147456, L_QX = 149504, L_RG = 157696;
constexpr int LDS_BYTES = 163840;

__global__ void __launch_bounds__(512, 2) k_qkv(const u16* __restrict__ x, const u16* __restrict__ W, const u16* __restrict__ wr,
                                                const u16* __restrict__ gw, char* __restrict__ ws) {
  extern __shared__ __attribute__((aligned(16))) char lds[];
  const unsigned l0 = (unsigned)(size_t)lds;
  int hg, rb; decode_block(blockIdx.x, hg, rb);
  const int lane = threadIdx.x & 63, w = __builtin_amdgcn_readfirstlane(threadIdx.x >> 6);
  const int rot = rb & 15;
  if (w == 4) glds16(wr + lane * 8, l0 + L_WV);
  if (w == 5) glds16(gw + lane * 8, l0 + L_WV + 1024);
#pragma unroll
  for (int i = 0; i < 8; ++i) {
    const int row = w * 8 + i;
    glds16(x + (size_t)(rb * 64 + row) * D_ + ((lane ^ (row & 15)) * 8), l0 + L_X + row * 1024);
  }
  const int wrow = lane >> 3;
  const u16* wsrc = W + (size_t)wrow * 1536 + hg * 64 + (((lane & 7) ^ (((wrow >> 1) & 1) << 2)) * 8);
  auto issue = [&](int r) {
    const int slot = r % 10;
    if (r < 16) {
      const int kb = (r + rot) & 15;
      glds16(wsrc + (size_t)(kb * 32 + (w & 3) * 8) * 1536 + (w < 4 ? 512 : 1024), l0 + L_W + slot * 8192 + w * 1024);
    } else {
      const int kq = (r - 16 + (rot >> 1)) & 7;
      glds16(wsrc + (size_t)(kq * 64 + w * 8) * 1536, l0 + L_W + slot * 8192 + w * 1024);
    }
  };
#pragma unroll
  for (int r = 0; r < 10; ++r) issue(r);

  const int c31 = lane & 31, hh = lane >> 5, blk = (lane >> 4) & 1, q4 = (lane & 15) >> 2, p4 = lane & 3;
  const unsigned xbase = l0 + L_X + c31 * 1024 + ((hh ^ (c31 & 15)) << 4);
  f32x16 acc0 = {0}, acc1 = {0};
  char* kvb = ws + WS_KV;
  if (w < 4) {
    const int part = w >> 1, pair = w & 1;
    const unsigned wbase = l0 + L_W + part * 4096 + (8 * hh + q4) * 128 + (((4 * pair + 2 * blk + (p4 >> 1)) ^ (((q4 >> 1) & 1) << 2)) << 4) + 8 * (p4 & 1);
#pragma unroll
    for (int g = 0; g < 8; ++g) {
      if (g == 0) WAIT_BAR(8); else WAIT_BAR(6);
      if (g >= 1) { issue(2 * g + 8); issue(2 * g + 9); }
#pragma unroll
      for (int rr = 0; rr < 2; ++rr) {
        const int r = 2 * g + rr, slot = r % 10, kb = (r + rot) & 15;
#pragma unroll
        for (int u = 0; u < 2; ++u) {
          const unsigned xa = (xbase ^ ((4 * (kb & 3) + 2 * u) << 4)) + (kb >> 2) * 256;
          const bf16x8 wf = cat8(lds_tr(wbase + slot * 8192 + 16 * u * 128), lds_tr(wbase + slot * 8192 + (16 * u + 4) * 128));
          const bf16x8 x0 = lds_b128(xa), x1 = lds_b128(xa + 32768);
          if (part == 0) { acc0 = MFMA32(wf, x0, acc0); acc1 = MFMA32(wf, x1, acc1); }
          else { acc0 = MFMA32(x0, wf, acc0); acc1 = MFMA32(x1, wf, acc1); }
        }
      }
    }
    const int s23 = (c31 & 19) | ((c31 & 4) << 1) | ((c31 & 8) >> 1);
#pragma unroll
    for (int t = 0; t < 2; ++t) {
      const f32x16 a = t ? acc1 : acc0;
#pragma unroll
      for (int z = 0; z < 2; ++z) {
        u32x4 v = {cvtpk(a[8 * z], a[8 * z + 1]), cvtpk(a[8 * z + 2], a[8 * z + 3]), cvtpk(a[8 * z + 4], a[8 * z + 5]), cvtpk(a[8 * z + 6], a[8 * z + 7])};
        size_t off;
        if (part == 0) off = (size_t)((4 * hg + 2 * pair + z) * 32 + rb) * 4096 + t * 2048 + (s23 + 32 * hh) * 16;
        else off = (size_t)((4 * hg + 2 * pair + blk) * 32 + rb) * 4096 + t * 2048 + 1024 + z * 512 + (hh * 16 + (lane & 15)) * 16;
        *(u32x4*)(kvb + off) = v;
      }
    }
    WAIT_BAR(4);
  } else {
    const int wq = w - 4;
#pragma unroll
    for (int g = 0; g < 8; ++g) {
      if (g == 0) WAIT_BAR(8); else WAIT_BAR(6);
      if (g >= 1) { issue(2 * g + 8); issue(2 * g + 9); }
      if (g == 0) {
        const int tok = 16 * wq + (lane & 15), g4 = lane >> 4;
        f32x4 d = {0};
#pragma unroll
        for (int u = 0; u < 16; ++u) {
          const bf16x8 a = lds_b128(l0 + L_WV + (lane & 1) * 1024 + (4 * u + g4) * 16);
          const bf16x8 b = lds_b128(l0 + L_X + tok * 1024 + (((4 * u + g4) ^ (tok & 15)) << 4));
          d = MFMA16(a, b, d);
        }
        if (lane < 16 && hg == 0) {
          const int token = rb * 64 + tok;
          const unsigned rb16 = cvtpk(d[0], 0.f) & 0xffffu;
          const unsigned srt = (rb16 & 0x8000u) ? (~rb16 & 0xffffu) : (rb16 | 0x8000u);
          ((u32*)(ws + WS_R))[token] = (srt << 11) | (2047 - token);
          ((float*)(ws + WS_G))[token] = bfround(sigmoidf_(bfround(d[1])));
        }
      }
    }
    WAIT_BAR(0);
    const int pair = wq & 1, tile = wq >> 1;
    const unsigned wbase = l0 + L_W + (8 * hh + q4) * 128 + (((4 * pair + 2 * blk + (p4 >> 1)) ^ (((q4 >> 1) & 1) << 2)) << 4) + 8 * (p4 & 1);
#pragma unroll
    for (int r = 16; r < 24; ++r) {
      const int slot = r % 10, kq = (r - 16 + (rot >> 1)) & 7;
#pragma unroll
      for (int u = 0; u < 4; ++u) {
        const unsigned xa = (xbase ^ ((8 * (kq & 1) + 2 * u) << 4)) + (kq >> 1) * 256 + tile * 32768;
        const bf16x8 wf = cat8(lds_tr(wbase + slot * 8192 + 16 * u * 128), lds_tr(wbase + slot * 8192 + (16 * u + 4) * 128));
        acc0 = MFMA32(wf, lds_b128(xa), acc0);
      }
    }
    const float qs = 0.25f * 1.4426950408889634f;
#pragma unroll
    for (int z = 0; z < 2; ++z) {
      u32x4 v = {cvtpk(acc0[8 * z] * qs, acc0[8 * z + 1] * qs), cvtpk(acc0[8 * z + 2] * qs, acc0[8 * z + 3] * qs),
                 cvtpk(acc0[8 * z + 4] * qs, acc0[8 * z + 5] * qs), cvtpk(acc0[8 * z + 6] * qs, acc0[8 * z + 7] * qs)};
      *(u32x4*)(ws + WS_QF + (size_t)((4 * hg + 2 * pair + z) * 64 + 2 * rb + tile) * 1024 + lane * 16) = v;
    }
  }
}

#define STR_(x) #x
#define STR(x) STR_(x)
#define EX1(b) "v_exp_f32 v[" STR(b) "], v[" STR(b) "]\n\t"
#define EX4(b) EX1(b) EX1(b+1) EX1(b+2) EX1(b+3)
#define AR4(b) "a[" STR(b) ":" STR(b) "+3]"
#define KS(s) AR4(40+4*s)
#define V0(s) AR4(56+8*s)
#define V1(s) AR4(60+8*s)
#define SMF(acc, va, sb) "v_smfmac_f32_32x32x32_bf16 " acc ", " va ", v[" STR(sb) ":" STR(sb) "+7], %[idx]\n\t"
#define OA "a[0:15]"
#define OB "a[16:31]"
#define QKA(s) "v_mfma_f32_32x32x16_bf16 v[64:79], " KS(s) ", a[32:35], v[96:111]\n\t"
#define QKB(s) "v_mfma_f32_32x32x16_bf16 v[80:95], " KS(s) ", a[36:39], v[112:127]\n\t"
#define LDK(s, so) "buffer_load_dwordx4 " KS(s) ", %[vk], %[rs], " so " offen sc1\n\t"
#define LDV(s, so) "s_mov_b64 exec, %[lm]\n\t" \
                   "buffer_load_dwordx4 " V0(s) ", %[vv], %[rs], " so " offen offset:1024 sc1\n\t" \
                   "buffer_load_dwordx4 " V1(s) ", %[vv], %[rs], " so " offen offset:1536 sc1\n\t" \
                   "s_mov_b64 exec, -1\n\t"
#define ACLOB "a0","a1","a2","a3","a4","a5","a6","a7","a8","a9","a10","a11","a12","a13","a14","a15","a16","a17","a18","a19", \
  "a20","a21","a22","a23","a24","a25","a26","a27","a28","a29","a30","a31","a32","a33","a34","a35","a36","a37","a38","a39", \
  "a40","a41","a42","a43","a44","a45","a46","a47","a48","a49","a50","a51","a52","a53","a54","a55","a56","a57","a58","a59", \
  "a60","a61","a62","a63","a64","a65","a66","a67","a68","a69","a70","a71","a72","a73","a74","a75","a76","a77","a78","a79", \
  "a80","a81","a82","a83","a84","a85","a86","a87"
#define TILE(s, sp, sn, soV, soK) \
  asm volatile(EX4(64) SMF(OB, V1(sp), 88) LDV(sp, "%[sv]") EX4(68) QKB(s) LDK(s, "%[sk]") EX4(72) \
               "s_waitcnt vmcnt(10)\n\t" SMF(OA, V0(s), 64) EX4(76) \
               EX4(80) SMF(OA, V1(s), 72) EX4(84) "s_waitcnt vmcnt(9)\n\t" QKA(sn) EX4(88) SMF(OB, V0(s), 80) EX4(92) \
               : "+{v[64:79]}"(sa), "+{v[80:95]}"(sb) \
               : "{v[96:111]}"(ma), "{v[112:127]}"(mb), [idx] "v"(idx), [vk] "v"(vk), [vv] "v"(vv), [rs] "s"(rs), [lm] "s"(lm), \
                 [sv] "s"(soV), [sk] "s"(soK) : "memory", ACLOB)

__device__ __forceinline__ float max16(const f32x16& p) {
  float a = fmaxf(fmaxf(p[0], p[1]), p[2]), b = fmaxf(fmaxf(p[3], p[4]), p[5]);
  a = fmaxf(fmaxf(a, p[6]), p[7]); b = fmaxf(fmaxf(b, p[8]), p[9]);
  a = fmaxf(fmaxf(a, p[10]), p[11]); b = fmaxf(fmaxf(b, p[12]), p[13]);
  a = fmaxf(fmaxf(a, p[14]), p[15]);
  const float m = fmaxf(a, b);
  auto rr = __builtin_amdgcn_permlane32_swap(__float_as_uint(m), __float_as_uint(m), false, false);
  return fmaxf(__uint_as_float(rr[0]), __uint_as_float(rr[1]));
}

__global__ void __launch_bounds__(512, 2) k_attn(char* __restrict__ ws) {
  __shared__ __attribute__((aligned(16))) float xch[8][9][64];
  int hg, rb; decode_block(blockIdx.x, hg, rb);
  const int lane = threadIdx.x & 63, w = __builtin_amdgcn_readfirstlane(threadIdx.x >> 6);
  const int j = w & 3, kh = w >> 2, head = 4 * hg + j;
  const __amdgpu_buffer_rsrc_t rs = __builtin_amdgcn_make_buffer_rsrc(ws, 0, (int)WS_END, 0x00020000);
  const unsigned idx = 0xDDDDDDDDu, vk = lane * 16, vv = ((lane >> 5) * 16 + (lane & 15)) * 16;
  const u64 lm = 0x0000FFFF0000FFFFull;
  const unsigned kv0 = (unsigned)WS_KV + head * 32 * 4096 + kh * 2048;
  auto so = [&](int t) { return kv0 + ((rb + t) & 31) * 4096; };
  f32x16 sa, sb, ma, mb;
  {
    const unsigned ones = 0x3F803F80u;
    const unsigned sq = (unsigned)WS_QF + (head * 64 + 2 * rb) * 1024, s0 = so(0), sm = s0 - kh * 2048;
#define W1(r) "v_accvgpr_write_b32 a[" STR(r) "], %[on]\n\t"
#define W4(r) W1(r) W1(r+1) W1(r+2) W1(r+3)
#define W16(r) W4(r) W4(r+4) W4(r+8) W4(r+12)
#define Z1(r) "v_accvgpr_write_b32 a[" STR(r) "], 0\n\t"
#define Z4(r) Z1(r) Z1(r+1) Z1(r+2) Z1(r+3)
#define Z16(r) Z4(r) Z4(r+4) Z4(r+8) Z4(r+12)
    asm volatile(W16(56) W16(72) Z16(0) Z16(16)
                 "buffer_load_dwordx4 a[32:35], %[vk], %[rs], %[sq] offen sc1\n\t"
                 "buffer_load_dwordx4 a[36:39], %[vk], %[rs], %[sq] offen offset:1024 sc1\n\t"
                 LDK(1, "%[sm]") LDK(0, "%[s0]") LDV(0, "%[s0]")
                 "s_waitcnt vmcnt(3)\n\t"
                 "v_mfma_f32_32x32x16_bf16 v[64:79], " KS(1) ", a[32:35], 0\n\t"
                 "v_mfma_f32_32x32x16_bf16 v[80:95], " KS(1) ", a[36:39], 0\n\t"
                 "s_nop 15\n\ts_nop 7\n\t"
                 : "={v[64:79]}"(sa), "={v[80:95]}"(sb)
                 : [on] "v"(ones), [vk] "v"(vk), [vv] "v"(vv), [rs] "s"(rs), [lm] "s"(lm), [sq] "s"(sq), [s0] "s"(s0), [sm] "s"(sm)
                 : "memory", ACLOB);
    const float nA = -max16(sa), nB = -max16(sb);
#pragma unroll
    for (int i = 0; i < 16; ++i) { ma[i] = nA; mb[i] = nB; }
    asm volatile("s_waitcnt vmcnt(0)\n\ts_nop 1\n\t" QKA(0) QKB(0) "s_nop 11\n\t"
                 EX4(64) EX4(68) EX4(72) SMF(OA, V0(0), 64) EX4(76) EX4(80) SMF(OA, V1(0), 72) EX4(84) EX4(88) SMF(OB, V0(0), 80) EX4(92)
                 : "=&{v[64:79]}"(sa), "=&{v[80:95]}"(sb)
                 : "{v[96:111]}"(ma), "{v[112:127]}"(mb), [idx] "v"(idx) : "memory", ACLOB);
    const unsigned s1 = so(1), s2 = so(2), s3 = so(3), s4 = so(4);
    asm volatile(LDK(1, "%[s1]") LDV(1, "%[s1]") LDK(2, "%[s2]") LDV(2, "%[s2]") LDK(3, "%[s3]") LDV(3, "%[s3]") LDK(0, "%[s4]")
                 "s_waitcnt vmcnt(9)\n\t" QKA(1) "s_nop 15\n\t"
                 : "=&{v[64:79]}"(sa)
                 : "{v[96:111]}"(ma), [vk] "v"(vk), [vv] "v"(vv), [rs] "s"(rs), [lm] "s"(lm), [s1] "s"(s1), [s2] "s"(s2), [s3] "s"(s3), [s4] "s"(s4)
                 : "memory", ACLOB);
  }
  TILE(1, 0, 2, so(4), so(5));
  TILE(2, 1, 3, so(5), so(6));
  TILE(3, 2, 0, so(6), so(7));
#pragma unroll 1
  for (int t = 4; t < 32; t += 4) {
    TILE(0, 3, 1, so(t + 3), so(t + 4));
    TILE(1, 0, 2, so(t + 4), so(t + 5));
    TILE(2, 1, 3, so(t + 5), so(t + 6));
    TILE(3, 2, 0, so(t + 6), so(t + 7));
  }
  float oa[9], ob[9];
#define RD(i, r) "v_accvgpr_read_b32 %" STR(i) ", a" STR(r) "\n\t"
  asm volatile("s_nop 1\n\t" SMF(OB, V1(3), 88) "s_waitcnt vmcnt(0)\n\ts_nop 15\n\ts_nop 3\n\t"
               RD(0, 0) RD(1, 1) RD(2, 2) RD(3, 3) RD(4, 4) RD(5, 5) RD(6, 6) RD(7, 7) RD(8, 8)
               RD(9, 16) RD(10, 17) RD(11, 18) RD(12, 19) RD(13, 20) RD(14, 21) RD(15, 22) RD(16, 23) RD(17, 24)
               : "=v"(oa[0]), "=v"(oa[1]), "=v"(oa[2]), "=v"(oa[3]), "=v"(oa[4]), "=v"(oa[5]), "=v"(oa[6]), "=v"(oa[7]), "=v"(oa[8]),
                 "=v"(ob[0]), "=v"(ob[1]), "=v"(ob[2]), "=v"(ob[3]), "=v"(ob[4]), "=v"(ob[5]), "=v"(ob[6]), "=v"(ob[7]), "=v"(ob[8])
               : "{v[80:95]}"(sb), [idx] "v"(idx) : "memory", ACLOB);
#pragma unroll
  for (int i = 0; i < 9; ++i) xch[w ^ 4][i][lane] = kh ? oa[i] : ob[i];
  __syncthreads();
  float o[9];
#pragma unroll
  for (int i = 0; i < 9; ++i) o[i] = (kh ? ob[i] : oa[i]) + xch[w][i][lane];
  const float rl = 1.f / o[8];
  u32x4 v = {cvtpk(o[0] * rl, o[1] * rl), cvtpk(o[2] * rl, o[3] * rl), cvtpk(o[4] * rl, o[5] * rl), cvtpk(o[6] * rl, o[7] * rl)};
  *(u32x4*)(ws + WS_OF + (size_t)((2 * rb + kh) * 32 + head) * 1024 + lane * 16) = v;
}

__global__ void __launch_bounds__(256) k_ffn1(const u16* __restrict__ x, const u16* __restrict__ wg, const u16* __restrict__ wu, char* __restrict__ ws) {
  __shared__ unsigned red[4]; __shared__ float part[2][32][8];
  const int tid = threadIdx.x;
  unsigned best = 0;
  for (int i = tid; i < S_; i += 256) best = max(best, ((const u32*)(ws + WS_R))[i]);
  for (int m = 32; m >= 1; m >>= 1) best = max(best, (unsigned)__shfl_xor((int)best, m));
  if ((tid & 63) == 0) red[tid >> 6] = best;
  __syncthreads();
  best = max(max(red[0], red[1]), max(red[2], red[3]));
  const int idx = 2047 - (best & 2047);
  if (blockIdx.x == 0 && tid == 0) ((u32*)(ws + WS_Y))[512] = best;
  const int c = tid & 7, kg = tid >> 3;
  float ag = 0.f, au = 0.f;
  for (int k = kg; k < D_; k += 32) {
    const float xv = bflo(x[(size_t)idx * D_ + k]);
    ag += xv * bflo(wg[(size_t)k * F_ + blockIdx.x * 8 + c]);
    au += xv * bflo(wu[(size_t)k * F_ + blockIdx.x * 8 + c]);
  }
  part[0][kg][c] = ag; part[1][kg][c] = au;
  __syncthreads();
  if (tid < 8) {
    float sg = 0.f, su = 0.f;
    for (int i = 0; i < 32; ++i) { sg += part[0][i][tid]; su += part[1][i][tid]; }
    sg = bfround(sg); su = bfround(su);
    const float gg = bfround(sg * sigmoidf_(sg));
    ((float*)(ws + WS_H))[blockIdx.x * 8 + tid] = bfround(gg * su);
  }
}
__global__ void __launch_bounds__(256) k_ffn2(const u16* __restrict__ wd, char* __restrict__ ws) {
  __shared__ float part[32][8];
  const int tid = threadIdx.x, c = tid & 7, kg = tid >> 3;
  float a = 0.f;
  for (int k = kg; k < F_; k += 32) a += ((const float*)(ws + WS_H))[k] * bflo(wd[(size_t)k * D_ + blockIdx.x * 8 + c]);
  part[kg][c] = a;
  __syncthreads();
  if (tid < 8) { float s = 0.f; for (int i = 0; i < 32; ++i) s += part[i][tid]; ((float*)(ws + WS_Y))[blockIdx.x * 8 + tid] = s; }
}

__global__ void __launch_bounds__(512, 2) k_out(const u16* __restrict__ x, const u16* __restrict__ wo, const char* __restrict__ ws, u16* __restrict__ out) {
  extern __shared__ __attribute__((aligned(16))) char lds[];
  const unsigned l0 = (unsigned)(size_t)lds;
  int hg, rb; decode_block(blockIdx.x, hg, rb);
  const int lane = threadIdx.x & 63, w = __builtin_amdgcn_readfirstlane(threadIdx.x >> 6);
  const int tt = 2 * rb + (hg >> 2), cg = hg & 3;
#pragma unroll
  for (int i = 0; i < 4; ++i) glds16(ws + WS_OF + (size_t)(tt * 32 + 4 * w + i) * 1024 + lane * 16, l0 + 131072 + (4 * w + i) * 1024);
  {
    const int r4 = lane >> 4;
    const u16* src = wo + (size_t)r4 * D_ + cg * 128 + (((lane & 15) ^ (r4 << 2)) * 8);
#pragma unroll
    for (int i = 0; i < 16; ++i) glds16(src + (size_t)(w * 64 + i * 4) * D_, l0 + (w * 64 + i * 4) * 256);
  }
  WAIT_BAR(0);
  if (w < 4) {
    const int nt = w, c31 = lane & 31, hh = lane >> 5, blk = (lane >> 4) & 1, q4 = (lane & 15) >> 2, p4 = lane & 3;
    const int token = tt * 32 + c31, col = cg * 128 + nt * 32 + hh * 16;
    const u32x4 xa = *(const u32x4*)(x + (size_t)token * D_ + col), xb = *(const u32x4*)(x + (size_t)token * D_ + col + 8);
    const float g = ((const float*)(ws + WS_G))[token];
    const unsigned best = ((const u32*)(ws + WS_Y))[512];
    const unsigned abase = l0 + (4 * hh + q4) * 256 + (((4 * nt + 2 * (p4 & 1) + blk) ^ (q4 << 2)) << 4) + 8 * (p4 >> 1);
    f32x16 acc = {0};
#pragma unroll
    for (int hd = 0; hd < 32; ++hd) {
      const bf16x8 a = cat8(lds_tr(abase + hd * 4096), lds_tr(abase + hd * 4096 + 2048));
      acc = MFMA32(a, lds_b128(l0 + 131072 + hd * 1024 + lane * 16), acc);
    }
    float b[16];
#pragma unroll
    for (int i = 0; i < 4; ++i) { b[2 * i] = bflo(xa[i]); b[2 * i + 1] = bfhi(xa[i]); b[8 + 2 * i] = bflo(xb[i]); b[9 + 2 * i] = bfhi(xb[i]); }
    if (token == 2047 - (int)(best & 2047)) {
      const unsigned srt = best >> 11;
      const unsigned r16 = (srt & 0x8000u) ? (srt & 0x7fffu) : (~srt & 0xffffu);
      const float rs = bfround(sigmoidf_(bflo(r16)));
#pragma unroll
      for (int i = 0; i < 16; ++i) b[i] = bfround(b[i] + bfround(bfround(((const float*)(ws + WS_Y))[col + i]) * rs));
    }
    const float og = bfround(1.f - g);
    float r[16];
#pragma unroll
    for (int i = 0; i < 16; ++i) r[i] = bfround(g * bfround(acc[i])) + bfround(og * b[i]);
    u32x4 o0 = {cvtpk(r[0], r[1]), cvtpk(r[2], r[3]), cvtpk(r[4], r[5]), cvtpk(r[6], r[7])};
    u32x4 o1 = {cvtpk(r[8], r[9]), cvtpk(r[10], r[11]), cvtpk(r[12], r[13]), cvtpk(r[14], r[15])};
    *(u32x4*)(out + (size_t)token * D_ + col) = o0;
    *(u32x4*)(out + (size_t)token * D_ + col + 8) = o1;
  }
}

extern "C" void kernel_launch(void* const* d_in, const int* in_sizes, int n_in, void* d_out, int out_size, void* d_ws, size_t ws_size,
                              hipStream_t stream) {
  const u16* x = (const u16*)d_in[0]; const u16* wqkv = (const u16*)d_in[1]; const u16* wo = (const u16*)d_in[2];
  const u16* wr = (const u16*)d_in[4]; const u16* wg = (const u16*)d_in[5]; const u16* wu = (const u16*)d_in[6];
  const u16* wd = (const u16*)d_in[7]; const u16* gw = (const u16*)d_in[8];
  char* ws = (char*)d_ws;
  (void)hipFuncSetAttribute((const void*)k_qkv, hipFuncAttributeMaxDynamicSharedMemorySize, LDS_BYTES);
  (void)hipFuncSetAttribute((const void*)k_out, hipFuncAttributeMaxDynamicSharedMemorySize, LDS_BYTES);
  k_qkv<<<256, 512, LDS_BYTES, stream>>>(x, wqkv, wr, gw, ws);
  k_ffn1<<<224, 256, 0, stream>>>(x, wg, wu, ws);
  k_ffn2<<<64, 256, 0, stream>>>(wd, ws);
  k_attn<<<256, 512, 0, stream>>>(ws);
  k_out<<<256, 512, LDS_BYTES, stream>>>(x, wo, ws, (u16*)d_out);
}
```

```cpp
#include <hip/hip_runtime.h>
#include <hip/hip_bf16.h>

typedef unsigned short u16;
typedef unsigned int u32;
typedef unsigned long long u64;
typedef __attribute__((ext_vector_type(8))) __bf16 bf16x8;
typedef __attribute__((ext_vector_type(16))) __bf16 bf16x16;
typedef __attribute__((ext_vector_type(16))) float f32x16;
typedef __attribute__((ext_vector_type(8))) float f32x8;
typedef __attribute__((ext_vector_type(4))) float f32x4;
typedef __attribute__((ext_vector_type(4))) unsigned u32x4;
typedef __attribute__((ext_vector_type(2))) unsigned u32x2;
typedef __attribute__((ext_vector_type(4))) short s16x4;
#define LAS __attribute__((address_space(3)))

constexpr int D_ = 512, F_ = 1792;
constexpr u32 KVB = 2176;
constexpr u32 WS_KV = 0;
constexpr u32 WS_OF = 8u << 20;
constexpr u32 WS_FL = 10u << 20;
constexpr u32 WS_OL = WS_FL + 32768;
constexpr u32 WS_H  = WS_OL + 32768;
constexpr u32 WS_Y  = WS_H + 16384;
constexpr u32 WS_EP = WS_Y + 65536;
constexpr u32 WS_END = WS_EP + 128;
constexpr u32 L_X = 0, L_W = 65536, L_KV = 131072, L_WV = 147456, L_QX = 149504, L_RG = 157696, L_ONE = 158208;
constexpr u32 L_GU = 0, L_WD = 16384, L_XS = 23552, L_HS = 24576;
constexpr u32 L_OS = 131072;
constexpr u32 L_CTL = 163808;
constexpr int LDS_BYTES = 163840;
constexpr int SPIN_MAX = 1 << 18;

#define STG(i)
#define ST(i) ST_(i)
#define ST_(i)
#define STDECL
#define STDUMP()
#define STARG
#define STPASS

#define HPRIO 3
#define MAP 3
#define MAPB(b, hg, rb) do { if (MAP == 3) { hg = 2 * (b & 3) + ((b >> 3) & 1); rb = 16 * ((b >> 2) & 1) + (b >> 4); } \
  else if (MAP == 2) { hg = 4 * (b & 1) + ((b >> 3) & 3); rb = 8 * ((b >> 1) & 3) + (b >> 5); } \
  else if (MAP == 1) { hg = b & 7; rb = b >> 3; } else { hg = (b >> 3) & 7; rb = 4 * (b & 7) + (b >> 6); } } while (0)
typedef __amdgpu_buffer_rsrc_t rsrc_t;
__device__ __forceinline__ rsrc_t mkrs(const void* p, unsigned bytes) { return __builtin_amdgcn_make_buffer_rsrc((void*)p, 0, (int)bytes, 0x00020000); }
template <unsigned ADD>
__device__ __forceinline__ void dma(unsigned voff, rsrc_t r, unsigned soff, unsigned m) {
  asm volatile("s_add_u32 m0, %0, %c4\n\ts_nop 0\n\tbuffer_load_dwordx4 %1, %2, %3 offen lds" :: "s"(m), "v"(voff), "s"(r), "s"(soff), "i"(ADD) : "memory", "scc");
}
__device__ __forceinline__ unsigned cvtpk(float lo, float hi) {
  typedef __attribute__((ext_vector_type(2))) __bf16 bf16x2;
  typedef __attribute__((ext_vector_type(2))) float f32x2;
  f32x2 v = {lo, hi};
  return __builtin_bit_cast(unsigned, __builtin_convertvector(v, bf16x2));
}
__device__ __forceinline__ float bfround(float f) { return __uint_as_float(cvtpk(f, 0.f) << 16); }
__device__ __forceinline__ float bflo(unsigned u) { return __uint_as_float(u << 16); }
__device__ __forceinline__ float bfhi(unsigned u) { return __uint_as_float(u & 0xffff0000u); }
__device__ __forceinline__ float sigmoidf_(float z) { return __builtin_amdgcn_rcpf(1.f + __expf(-z)); }
__device__ __forceinline__ bf16x8 lds_b128(unsigned a) { return *(const LAS bf16x8*)(size_t)a; }
__device__ __forceinline__ void lds_w128(unsigned a, u32x4 v) { *(LAS u32x4*)(size_t)a = v; }
__device__ __forceinline__ s16x4 lds_tr(unsigned a) { return __builtin_amdgcn_ds_read_tr16_b64_v4i16((LAS s16x4*)(size_t)a); }
__device__ __forceinline__ bf16x8 cat8(s16x4 a, s16x4 b) {
  typedef __attribute__((ext_vector_type(8))) short s16x8;
  s16x8 r = {a[0], a[1], a[2], a[3], b[0], b[1], b[2], b[3]};
  return __builtin_bit_cast(bf16x8, r);
}
__device__ __forceinline__ u32x4 pack8(const f32x16& a, int z, float s) {
  return u32x4{cvtpk(a[8 * z] * s, a[8 * z + 1] * s), cvtpk(a[8 * z + 2] * s, a[8 * z + 3] * s),
               cvtpk(a[8 * z + 4] * s, a[8 * z + 5] * s), cvtpk(a[8 * z + 6] * s, a[8 * z + 7] * s)};
}
__device__ __forceinline__ u32x4 pack8(const f32x16& a, int z) {
  return u32x4{cvtpk(a[8 * z], a[8 * z + 1]), cvtpk(a[8 * z + 2], a[8 * z + 3]), cvtpk(a[8 * z + 4], a[8 * z + 5]), cvtpk(a[8 * z + 6], a[8 * z + 7])};
}
__device__ __forceinline__ unsigned wave_max_u32(unsigned v) {
#pragma unroll
  for (int m = 32; m >= 1; m >>= 1) v = max(v, (unsigned)__shfl_xor((int)v, m));
  return __builtin_amdgcn_readfirstlane(v);
}
#define MFMA32(a, b, c) __builtin_amdgcn_mfma_f32_32x32x16_bf16(a, b, c, 0, 0, 0)
#define MFMA16(a, b, c) __builtin_amdgcn_mfma_f32_16x16x32_bf16(a, b, c, 0, 0, 0)
#define WAIT_BAR(N) asm volatile("s_waitcnt vmcnt(" #N ") lgkmcnt(0)\n\ts_barrier" ::: "memory")
#define BAR() asm volatile("s_waitcnt lgkmcnt(0)\n\ts_barrier" ::: "memory")
#define DRAIN() asm volatile("s_waitcnt vmcnt(0)" ::: "memory")
#define SBAR() __builtin_amdgcn_sched_barrier(0)
#define FRESH() asm volatile("" ::: "memory")
#define LDSU(a) (*(volatile LAS u32*)(size_t)(a))
#define LD128(off) __builtin_amdgcn_raw_buffer_load_b128(rs, (off), 0, 16)
#define LD64(off) __builtin_amdgcn_raw_buffer_load_b64(rs, (off), 0, 16)
#define ST128(v, off) __builtin_amdgcn_raw_buffer_store_b128((v), rs, (off), 0, 16)
#define ST64(v, off) __builtin_amdgcn_raw_buffer_store_b64((v), rs, (off), 0, 16)

#define STR_(x) #x
#define STR(x) STR_(x)
#define EX1(b) "v_exp_f32 v[" STR(b) "], v[" STR(b) "]\n\t"
#define EX4(b) EX1(b) EX1(b+1) EX1(b+2) EX1(b+3)
#define AR4(b) "a[" STR(b) ":" STR(b) "+3]"
#define KS(s) AR4(32+4*s)
#define V0(s) AR4(48+8*s)
#define V1(s) AR4(52+8*s)
#define SMF(acc, va, sb) "v_smfmac_f32_32x32x32_bf16 " acc ", " va ", v[" STR(sb) ":" STR(sb) "+7], %[idx]\n\t"
#define OA "a[0:15]"
#define OB "a[16:31]"
#define QKA(s) "v_mfma_f32_32x32x16_bf16 v[36:51], " KS(s) ", %[qa], v[68:83]\n\t"
#define QKB(s) "v_mfma_f32_32x32x16_bf16 v[52:67], " KS(s) ", %[qb], v[68:83]\n\t"
#define LDK(s, so) "buffer_load_dwordx4 " KS(s) ", %[vk], %[rs], " so " offen sc1\n\t"
#define LDV(s, so) "buffer_load_dwordx4 " V0(s) ", %[v0], %[rs], " so " offen sc1\n\t" \
                   "buffer_load_dwordx4 " V1(s) ", %[v1], %[rs], " so " offen sc1\n\t"
#define ACLOB "a0","a1","a2","a3","a4","a5","a6","a7","a8","a9","a10","a11","a12","a13","a14","a15","a16","a17","a18","a19", \
  "a20","a21","a22","a23","a24","a25","a26","a27","a28","a29","a30","a31","a32","a33","a34","a35","a36","a37","a38","a39", \
  "a40","a41","a42","a43","a44","a45","a46","a47","a48","a49","a50","a51","a52","a53","a54","a55","a56","a57","a58","a59", \
  "a60","a61","a62","a63","a64","a65","a66","a67","a68","a69","a70","a71","a72","a73","a74","a75","a76","a77","a78","a79"
#define TILE(s, sp, sn, soV, soK) \
  asm volatile(EX4(36) SMF(OB, V1(sp), 60) LDV(sp, "%[sv]") EX4(40) QKB(s) LDK(s, "%[sk]") EX4(44) \
               "s_waitcnt vmcnt(9)\n\t" SMF(OA, V0(s), 36) EX4(48) \
               EX4(52) SMF(OA, V1(s), 44) EX4(56) QKA(sn) EX4(60) SMF(OB, V0(s), 52) EX4(64) \
               : "+{v[36:51]}"(sa), "+{v[52:67]}"(sb) \
               : "{v[68:83]}"(mm), [qa] "v"(qa), [qb] "v"(qb), [idx] "v"(idx), [vk] "v"(vk), [v0] "v"(vv0), [v1] "v"(vv1), [rs] "s"(rs), \
                 [sv] "s"(soV), [sk] "s"(soK) : "memory", ACLOB)

__device__ __forceinline__ float max16(const f32x16& p) {
  float a = fmaxf(fmaxf(p[0], p[1]), p[2]), b = fmaxf(fmaxf(p[3], p[4]), p[5]);
  a = fmaxf(fmaxf(a, p[6]), p[7]); b = fmaxf(fmaxf(b, p[8]), p[9]);
  a = fmaxf(fmaxf(a, p[10]), p[11]); b = fmaxf(fmaxf(b, p[12]), p[13]);
  a = fmaxf(fmaxf(a, p[14]), p[15]);
  return fmaxf(a, b);
}
__device__ __forceinline__ float halves_max(float m) {
  auto rr = __builtin_amdgcn_permlane32_swap(__float_as_uint(m), __float_as_uint(m), false, false);
  return fmaxf(__uint_as_float(rr[0]), __uint_as_float(rr[1]));
}

__device__ __forceinline__ void TAIL(char* ws, const u16* x, u16* out, __amdgpu_buffer_rsrc_t rs, int b, int w,
    const float (&oa)[9], const float (&ob)[9], bf16x8 qa, bf16x8 qb, unsigned vk, unsigned vv0, unsigned vv1, unsigned idx,
    float gate, u32 tlo, u32 thi STARG) {
  constexpr unsigned l0 = 0;
  asm volatile("" : "+v"(vk));
  const int lane = vk >> 4;
  int hg, rb; MAPB(b, hg, rb);
  const int c31 = lane & 31, hh = lane >> 5, blk = (lane >> 4) & 1, q4 = (lane & 15) >> 2, p4 = lane & 3;
  const int tt = 2 * rb + (hg >> 2), cg = hg & 3, j = w & 3, kh = w >> 2, head = 4 * hg + j;
#pragma unroll
  for (int i = 0; i < 9; ++i) *(LAS float*)(size_t)(l0 + L_OS + (w ^ 4) * 4096 + i * 256 + lane * 4) = kh ? oa[i] : ob[i];
  unsigned best = 0;
  for (int spin = 0; spin < (SPIN_MAX << 4) && !best; ++spin) best = __builtin_amdgcn_readfirstlane(LDSU(L_CTL + 12));
  best &= 0x7fffffffu;
  BAR();
  float o[9];
  bool bad = false;
#pragma unroll
  for (int i = 0; i < 9; ++i) {
    o[i] = (kh ? ob[i] : oa[i]) + *(const LAS float*)(size_t)(l0 + L_OS + w * 4096 + i * 256 + lane * 4);
    bad |= !(fabsf(o[i]) < 1e30f);
  }
  bad |= !(o[8] > 1e-18f && o[8] < 1e18f);
  if (__any(bad)) {
    const bf16x8 q = kh ? qb : qa;
    const unsigned kb0 = WS_KV + head * 64 * KVB;
    float m = -3.0e38f;
#pragma unroll 1
    for (int t = 0; t < 64; ++t) {
      const f32x16 s = MFMA32(__builtin_bit_cast(bf16x8, LD128(kb0 + t * KVB + vk)), q, (f32x16{0}));
      m = fmaxf(m, max16(s));
    }
    m = -halves_max(m);
    f32x16 c, oc = {0};
#pragma unroll
    for (int i = 0; i < 16; ++i) c[i] = m;
#pragma unroll 1
    for (int t = 0; t < 64; ++t) {
      const f32x16 s = MFMA32(__builtin_bit_cast(bf16x8, LD128(kb0 + t * KVB + vk)), q, c);
      f32x8 e0, e1;
#pragma unroll
      for (int i = 0; i < 8; ++i) { e0[i] = __builtin_amdgcn_exp2f(s[i]); e1[i] = __builtin_amdgcn_exp2f(s[8 + i]); }
      const u32x4 va = __builtin_amdgcn_raw_buffer_load_b128(rs, vv0, kb0 + t * KVB, 16);
      const u32x4 vb = __builtin_amdgcn_raw_buffer_load_b128(rs, vv1, kb0 + t * KVB, 16);
      oc = __builtin_amdgcn_smfmac_f32_32x32x32_bf16(__builtin_bit_cast(bf16x8, va), __builtin_bit_cast(bf16x16, e0), oc, (int)idx, 0, 0);
      oc = __builtin_amdgcn_smfmac_f32_32x32x32_bf16(__builtin_bit_cast(bf16x8, vb), __builtin_bit_cast(bf16x16, e1), oc, (int)idx, 0, 0);
    }
#pragma unroll
    for (int i = 0; i < 9; ++i) o[i] = oc[i];
  }
  {
    const float rl = 1.f / o[8];
    const unsigned lim = 0xfffffffeu;
    ST128((u32x4{min(cvtpk(o[0] * rl, o[1] * rl), lim), min(cvtpk(o[2] * rl, o[3] * rl), lim), min(cvtpk(o[4] * rl, o[5] * rl), lim),
                 min(cvtpk(o[6] * rl, o[7] * rl), lim)}), WS_OF + ((2 * rb + kh) * 32 + head) * 1024 + vk);
  }
  ST(8);

  const int token = tt * 32 + c31, col = cg * 128 + (w & 3) * 32 + hh * 16;
  const int sel = 2047 - (int)(best & 2047u);
  u32x4 xa, xc;
  float yv = 0.f;
  const unsigned abase = (4 * hh + q4) * 256 + (((4 * (w & 3) + 2 * (p4 & 1) + blk) ^ (q4 << 2)) << 4) + 8 * (p4 >> 1);
  if (w < 4) {
    asm volatile(
      "ds_read_b64_tr_b16 a[0:1], %0 offset:0\n\t"
      "ds_read_b64_tr_b16 a[2:3], %0 offset:2048\n\t"
      "ds_read_b64_tr_b16 a[4:5], %0 offset:4096\n\t"
      "ds_read_b64_tr_b16 a[6:7], %0 offset:6144\n\t"
      "ds_read_b64_tr_b16 a[8:9], %0 offset:8192\n\t"
      "ds_read_b64_tr_b16 a[10:11], %0 offset:10240\n\t"
      "ds_read_b64_tr_b16 a[12:13], %0 offset:12288\n\t"
      "ds_read_b64_tr_b16 a[14:15], %0 offset:14336\n\t"
      "ds_read_b64_tr_b16 a[16:17], %0 offset:16384\n\t"
      "ds_read_b64_tr_b16 a[18:19], %0 offset:18432\n\t"
      "ds_read_b64_tr_b16 a[20:21], %0 offset:20480\n\t"
      "ds_read_b64_tr_b16 a[22:23], %0 offset:22528\n\t"
      "ds_read_b64_tr_b16 a[24:25], %0 offset:24576\n\t"
      "ds_read_b64_tr_b16 a[26:27], %0 offset:26624\n\t"
      "ds_read_b64_tr_b16 a[28:29], %0 offset:28672\n\t"
      "ds_read_b64_tr_b16 a[30:31], %0 offset:30720\n\t"
      "ds_read_b64_tr_b16 a[32:33], %0 offset:32768\n\t"
      "ds_read_b64_tr_b16 a[34:35], %0 offset:34816\n\t"
      "ds_read_b64_tr_b16 a[36:37], %0 offset:36864\n\t"
      "ds_read_b64_tr_b16 a[38:39], %0 offset:38912\n\t"
      "ds_read_b64_tr_b16 a[40:41], %0 offset:40960\n\t"
      "ds_read_b64_tr_b16 a[42:43], %0 offset:43008\n\t"
      "ds_read_b64_tr_b16 a[44:45], %0 offset:45056\n\t"
      "ds_read_b64_tr_b16 a[46:47], %0 offset:47104\n\t"
      "ds_read_b64_tr_b16 a[48:49], %0 offset:49152\n\t"
      "ds_read_b64_tr_b16 a[50:51], %0 offset:51200\n\t"
      "ds_read_b64_tr_b16 a[52:53], %0 offset:53248\n\t"
      "ds_read_b64_tr_b16 a[54:55], %0 offset:55296\n\t"
      "ds_read_b64_tr_b16 a[56:57], %0 offset:57344\n\t"
      "ds_read_b64_tr_b16 a[58:59], %0 offset:59392\n\t"
      "ds_read_b64_tr_b16 a[60:61], %0 offset:61440\n\t"
      "ds_read_b64_tr_b16 a[62:63], %0 offset:63488\n\t"
      "ds_read_b64_tr_b16 a[64:65], %1 offset:0\n\t"
      "ds_read_b64_tr_b16 a[66:67], %1 offset:2048\n\t"
      "ds_read_b64_tr_b16 a[68:69], %1 offset:4096\n\t"
      "ds_read_b64_tr_b16 a[70:71], %1 offset:6144\n\t"
      "ds_read_b64_tr_b16 a[72:73], %1 offset:8192\n\t"
      "ds_read_b64_tr_b16 a[74:75], %1 offset:10240\n\t"
      "ds_read_b64_tr_b16 a[76:77], %1 offset:12288\n\t"
      "ds_read_b64_tr_b16 a[78:79], %1 offset:14336\n\t"
      :: "v"(abase), "v"(abase + 65536) : "memory", ACLOB);
    xa = *(const u32x4*)(x + (size_t)token * D_ + col); xc = *(const u32x4*)(x + (size_t)token * D_ + col + 8);
    if ((sel >> 5) == tt) {
      for (int spin = 0; spin < SPIN_MAX; ++spin) {
      if (spin) __builtin_amdgcn_s_sleep(4);
      FRESH();
        bool ok = true; yv = 0.f;
#pragma unroll
        for (int i = 0; i < 4; ++i) {
          const u32x4 a = LD128(WS_Y + ((4 * hh + i) * 512 + cg * 128 + w * 32 + c31) * 16);
          ok &= a[1] == thi && a[3] == thi && ((a[0] ^ tlo) | (a[2] ^ tlo)) < 0x10000u;
          yv += __uint_as_float((a[0] & 0xffffu) | (a[2] << 16));
        }
        if (__all(ok)) break;
      }
      yv += __shfl_xor(yv, 32);
    }
  }
  float tb[16];
  if (w < 4) {
#pragma unroll
    for (int i = 0; i < 4; ++i) { tb[2 * i] = bflo(xa[i]); tb[2 * i + 1] = bfhi(xa[i]); tb[8 + 2 * i] = bflo(xc[i]); tb[9 + 2 * i] = bfhi(xc[i]); }
    if ((sel >> 5) == tt) {
      const unsigned srt = best >> 11;
      const float rsel = bfround(sigmoidf_(bflo((srt & 0x8000u) ? (srt & 0x7fffu) : (~srt & 0xffffu))));
      const float yy = bfround(bfround(yv) * rsel);
#pragma unroll
      for (int i = 0; i < 16; ++i) { const float t = __shfl(yy, hh * 16 + i); if (token == sel) tb[i] = bfround(tb[i] + t); }
    }
    const float og = bfround(1.f - gate);
#pragma unroll
    for (int i = 0; i < 16; ++i) tb[i] *= og;
  }
  {
    u32x4 v[4];
    for (int spin = 0; spin < SPIN_MAX; ++spin) {
      if (spin) __builtin_amdgcn_s_sleep(4);
      FRESH();
      unsigned m = 0;
#pragma unroll
      for (int i = 0; i < 4; ++i) v[i] = LD128(WS_OF + (tt * 32 + 4 * w + i) * 1024 + vk);
#pragma unroll
      for (int i = 0; i < 4; ++i) m = max(max(m, max(v[i][0], v[i][1])), max(v[i][2], v[i][3]));
      if (!__any(m == ~0u)) break;
    }
    ST(9);
#pragma unroll
    for (int i = 0; i < 4; ++i) lds_w128(l0 + L_OS + (4 * w + i) * 1024 + vk, v[i]);
  }
  BAR();
  ST(10);
  if (w < 4) {
    f32x16 a;
    asm volatile(
      "ds_read_b128 v[20:23], %[b] offset:0\n\t"
      "ds_read_b128 v[24:27], %[b] offset:1024\n\t"
      "ds_read_b128 v[28:31], %[b] offset:2048\n\t"
      "ds_read_b128 v[32:35], %[b] offset:3072\n\t"
      "ds_read_b128 v[36:39], %[b] offset:4096\n\t"
      "ds_read_b128 v[40:43], %[b] offset:5120\n\t"
      "ds_read_b128 v[44:47], %[b] offset:6144\n\t"
      "ds_read_b128 v[48:51], %[b] offset:7168\n\t"
      "s_waitcnt lgkmcnt(7)\n\t"
      "v_mfma_f32_32x32x16_bf16 v[68:83], a[0:3], v[20:23], 0\n\t"
      "ds_read_b128 v[20:23], %[b] offset:8192\n\t"
      "ds_read_b64_tr_b16 a[0:1], %[a1] offset:16384\n\t"
      "ds_read_b64_tr_b16 a[2:3], %[a1] offset:18432\n\t"
      "s_waitcnt lgkmcnt(9)\n\t"
      "v_mfma_f32_32x32x16_bf16 v[68:83], a[4:7], v[24:27], v[68:83]\n\t"
      "ds_read_b128 v[24:27], %[b] offset:9216\n\t"
      "ds_read_b64_tr_b16 a[4:5], %[a1] offset:20480\n\t"
      "ds_read_b64_tr_b16 a[6:7], %[a1] offset:22528\n\t"
      "s_waitcnt lgkmcnt(11)\n\t"
      "v_mfma_f32_32x32x16_bf16 v[68:83], a[8:11], v[28:31], v[68:83]\n\t"
      "ds_read_b128 v[28:31], %[b] offset:10240\n\t"
      "ds_read_b64_tr_b16 a[8:9], %[a1] offset:24576\n\t"
      "ds_read_b64_tr_b16 a[10:11], %[a1] offset:26624\n\t"
      "s_waitcnt lgkmcnt(13)\n\t"
      "v_mfma_f32_32x32x16_bf16 v[68:83], a[12:15], v[32:35], v[68:83]\n\t"
      "ds_read_b128 v[32:35], %[b] offset:11264\n\t"
      "ds_read_b64_tr_b16 a[12:13], %[a1] offset:28672\n\t"
      "ds_read_b64_tr_b16 a[14:15], %[a1] offset:30720\n\t"
      "s_waitcnt lgkmcnt(15)\n\t"
      "v_mfma_f32_32x32x16_bf16 v[68:83], a[16:19], v[36:39], v[68:83]\n\t"
      "ds_read_b128 v[36:39], %[b] offset:12288\n\t"
      "ds_read_b64_tr_b16 a[16:17], %[a1] offset:32768\n\t"
      "ds_read_b64_tr_b16 a[18:19], %[a1] offset:34816\n\t"
      "s_waitcnt lgkmcnt(15)\n\t"
      "v_mfma_f32_32x32x16_bf16 v[68:83], a[20:23], v[40:43], v[68:83]\n\t"
      "ds_read_b128 v[40:43], %[b] offset:13312\n\t"
      "ds_read_b64_tr_b16 a[20:21], %[a1] offset:36864\n\t"
      "ds_read_b64_tr_b16 a[22:23], %[a1] offset:38912\n\t"
      "s_waitcnt lgkmcnt(15)\n\t"
      "v_mfma_f32_32x32x16_bf16 v[68:83], a[24:27], v[44:47], v[68:83]\n\t"
      "ds_read_b128 v[44:47], %[b] offset:14336\n\t"
      "ds_read_b64_tr_b16 a[24:25], %[a1] offset:40960\n\t"
      "ds_read_b64_tr_b16 a[26:27], %[a1] offset:43008\n\t"
      "s_waitcnt lgkmcnt(15)\n\t"
      "v_mfma_f32_32x32x16_bf16 v[68:83], a[28:31], v[48:51], v[68:83]\n\t"
      "ds_read_b128 v[48:51], %[b] offset:15360\n\t"
      "ds_read_b64_tr_b16 a[28:29], %[a1] offset:45056\n\t"
      "ds_read_b64_tr_b16 a[30:31], %[a1] offset:47104\n\t"
      "s_waitcnt lgkmcnt(15)\n\t"
      "v_mfma_f32_32x32x16_bf16 v[68:83], a[32:35], v[20:23], v[68:83]\n\t"
      "ds_read_b128 v[20:23], %[b] offset:16384\n\t"
      "ds_read_b64_tr_b16 a[32:33], %[a1] offset:49152\n\t"
      "ds_read_b64_tr_b16 a[34:35], %[a1] offset:51200\n\t"
      "s_waitcnt lgkmcnt(15)\n\t"
      "v_mfma_f32_32x32x16_bf16 v[68:83], a[36:39], v[24:27], v[68:83]\n\t"
      "ds_read_b128 v[24:27], %[b] offset:17408\n\t"
      "ds_read_b64_tr_b16 a[36:37], %[a1] offset:53248\n\t"
      "ds_read_b64_tr_b16 a[38:39], %[a1] offset:55296\n\t"
      "s_waitcnt lgkmcnt(15)\n\t"
      "v_mfma_f32_32x32x16_bf16 v[68:83], a[40:43], v[28:31], v[68:83]\n\t"
      "ds_read_b128 v[28:31], %[b] offset:18432\n\t"
      "ds_read_b64_tr_b16 a[40:41], %[a1] offset:57344\n\t"
      "ds_read_b64_tr_b16 a[42:43], %[a1] offset:59392\n\t"
      "s_waitcnt lgkmcnt(15)\n\t"
      "v_mfma_f32_32x32x16_bf16 v[68:83], a[44:47], v[32:35], v[68:83]\n\t"
      "ds_read_b128 v[32:35], %[b] offset:19456\n\t"
      "ds_read_b64_tr_b16 a[44:45], %[a1] offset:61440\n\t"
      "ds_read_b64_tr_b16 a[46:47], %[a1] offset:63488\n\t"
      "s_waitcnt lgkmcnt(15)\n\t"
      "v_mfma_f32_32x32x16_bf16 v[68:83], a[48:51], v[36:39], v[68:83]\n\t"
      "ds_read_b128 v[36:39], %[b] offset:20480\n\t"
      "s_waitcnt lgkmcnt(15)\n\t"
      "v_mfma_f32_32x32x16_bf16 v[68:83], a[52:55], v[40:43], v[68:83]\n\t"
      "ds_read_b128 v[40:43], %[b] offset:21504\n\t"
      "s_waitcnt lgkmcnt(15)\n\t"
      "v_mfma_f32_32x32x16_bf16 v[68:83], a[56:59], v[44:47], v[68:83]\n\t"
      "ds_read_b128 v[44:47], %[b] offset:22528\n\t"
      "s_waitcnt lgkmcnt(15)\n\t"
      "v_mfma_f32_32x32x16_bf16 v[68:83], a[60:63], v[48:51], v[68:83]\n\t"
      "ds_read_b128 v[48:51], %[b] offset:23552\n\t"
      "s_waitcnt lgkmcnt(15)\n\t"
      "v_mfma_f32_32x32x16_bf16 v[68:83], a[64:67], v[20:23], v[68:83]\n\t"
      "ds_read_b128 v[20:23], %[b] offset:24576\n\t"
      "s_waitcnt lgkmcnt(13)\n\t"
      "v_mfma_f32_32x32x16_bf16 v[68:83], a[68:71], v[24:27], v[68:83]\n\t"
      "ds_read_b128 v[24:27], %[b] offset:25600\n\t"
      "s_waitcnt lgkmcnt(11)\n\t"
      "v_mfma_f32_32x32x16_bf16 v[68:83], a[72:75], v[28:31], v[68:83]\n\t"
      "ds_read_b128 v[28:31], %[b] offset:26624\n\t"
      "s_waitcnt lgkmcnt(9)\n\t"
      "v_mfma_f32_32x32x16_bf16 v[68:83], a[76:79], v[32:35], v[68:83]\n\t"
      "ds_read_b128 v[32:35], %[b] offset:27648\n\t"
      "s_waitcnt lgkmcnt(7)\n\t"
      "v_mfma_f32_32x32x16_bf16 v[68:83], a[0:3], v[36:39], v[68:83]\n\t"
      "ds_read_b128 v[36:39], %[b] offset:28672\n\t"
      "s_waitcnt lgkmcnt(7)\n\t"
      "v_mfma_f32_32x32x16_bf16 v[68:83], a[4:7], v[40:43], v[68:83]\n\t"
      "ds_read_b128 v[40:43], %[b] offset:29696\n\t"
      "s_waitcnt lgkmcnt(7)\n\t"
      "v_mfma_f32_32x32x16_bf16 v[68:83], a[8:11], v[44:47], v[68:83]\n\t"
      "ds_read_b128 v[44:47], %[b] offset:30720\n\t"
      "s_waitcnt lgkmcnt(7)\n\t"
      "v_mfma_f32_32x32x16_bf16 v[68:83], a[12:15], v[48:51], v[68:83]\n\t"
      "ds_read_b128 v[48:51], %[b] offset:31744\n\t"
      "s_waitcnt lgkmcnt(7)\n\t"
      "v_mfma_f32_32x32x16_bf16 v[68:83], a[16:19], v[20:23], v[68:83]\n\t"
      "s_waitcnt lgkmcnt(6)\n\t"
      "v_mfma_f32_32x32x16_bf16 v[68:83], a[20:23], v[24:27], v[68:83]\n\t"
      "s_waitcnt lgkmcnt(5)\n\t"
      "v_mfma_f32_32x32x16_bf16 v[68:83], a[24:27], v[28:31], v[68:83]\n\t"
      "s_waitcnt lgkmcnt(4)\n\t"
      "v_mfma_f32_32x32x16_bf16 v[68:83], a[28:31], v[32:35], v[68:83]\n\t"
      "s_waitcnt lgkmcnt(3)\n\t"
      "v_mfma_f32_32x32x16_bf16 v[68:83], a[32:35], v[36:39], v[68:83]\n\t"
      "s_waitcnt lgkmcnt(2)\n\t"
      "v_mfma_f32_32x32x16_bf16 v[68:83], a[36:39], v[40:43], v[68:83]\n\t"
      "s_waitcnt lgkmcnt(1)\n\t"
      "v_mfma_f32_32x32x16_bf16 v[68:83], a[40:43], v[44:47], v[68:83]\n\t"
      "s_waitcnt lgkmcnt(0)\n\t"
      "v_mfma_f32_32x32x16_bf16 v[68:83], a[44:47], v[48:51], v[68:83]\n\t"
      "s_nop 15\n\t"
      "s_nop 3\n\t"
      : "=&{v[68:83]}"(a) : [b] "v"(L_OS + vk), [a1] "v"(abase + 65536) : "memory", ACLOB, "v20","v21","v22","v23","v24","v25","v26","v27","v28","v29","v30","v31","v32","v33","v34","v35","v36","v37","v38","v39","v40","v41","v42","v43","v44","v45","v46","v47","v48","v49","v50","v51");
    float r[16];
#pragma unroll
    for (int i = 0; i < 16; ++i) r[i] = fmaf(gate, a[i], tb[i]);
    *(u32x4*)(out + (size_t)token * D_ + col) = u32x4{cvtpk(r[0], r[1]), cvtpk(r[2], r[3]), cvtpk(r[4], r[5]), cvtpk(r[6], r[7])};
    *(u32x4*)(out + (size_t)token * D_ + col + 8) = u32x4{cvtpk(r[8], r[9]), cvtpk(r[10], r[11]), cvtpk(r[12], r[13]), cvtpk(r[14], r[15])};
    if (b == 0 && w == 0 && lane == 0) ST64((u32x2{tlo, thi}), WS_EP);
  }
  ST(11);
}

__global__ void __launch_bounds__(576, 3)
fused(const u16* __restrict__ x, const u16* __restrict__ W, const u16* __restrict__ wo, const u16* __restrict__ wr,
      const u16* __restrict__ wg, const u16* __restrict__ wu, const u16* __restrict__ wd, const u16* __restrict__ gw,
      char* __restrict__ ws, u16* __restrict__ out) {
  constexpr unsigned l0 = 0;
  const int b = blockIdx.x;
  int hg, rb; MAPB(b, hg, rb);
  const int lane = threadIdx.x & 63, w = __builtin_amdgcn_readfirstlane(threadIdx.x >> 6);
  const int c31 = lane & 31, hh = lane >> 5, blk = (lane >> 4) & 1, q4 = (lane & 15) >> 2, p4 = lane & 3;
  const int tt = 2 * rb + (hg >> 2), cg = hg & 3;
  const __amdgpu_buffer_rsrc_t rs = __builtin_amdgcn_make_buffer_rsrc(ws, 0, (int)WS_END, 0x00020000);
  STDECL
  ST(0);
  asm volatile("" :: "s"(x), "s"(W), "s"(wr), "s"(gw), "s"(ws));
#define GET_TAG() u64 ep_; asm volatile("s_load_dwordx2 %0, %1, 0x0\n\ts_waitcnt lgkmcnt(0)" : "=s"(ep_) : "s"(ws + WS_EP) : "memory"); \
  const u64 tag = ep_ * 0x9E3779B97F4A7C15ull + 0xD1B54A32D192ED03ull; const u32 thi = (u32)(tag >> 32), tlo = (u32)tag

  if (w == 8) {
#pragma unroll
    for (int i = 0; i < 8; ++i) ST128((u32x4{~0u, ~0u, ~0u, ~0u}), WS_OF + ((2 * rb + (i >> 2)) * 32 + 4 * hg + (i & 3)) * 1024 + lane * 16);
    if (lane < 4) LDSU(L_CTL + lane * 4) = 0;
    GET_TAG();
#pragma unroll 1
    for (int i = 0; i < 6; ++i) BAR();
    DRAIN();
    if (lane == 0) ST64((u32x2{tlo, thi}), WS_OL + (rb * 8 + hg) * 128);
#pragma unroll 1
    for (int i = 0; i < 5; ++i) BAR();
    const int cg1 = (b & 7) * 28 + (b >> 3);
    const int ks2 = b & 7, cg2 = b >> 3;
    const bool has1 = (b >> 3) < 28;
    ST(2);
    __builtin_amdgcn_s_setprio(HPRIO);
    const rsrc_t ro = mkrs(wo, D_ * D_ * 2);
    const unsigned vwo = (lane >> 4) * (D_ * 2) + cg * 256 + (((lane & 15) ^ ((lane >> 4) << 2)) * 16);
    unsigned key = 0;
    for (int spin = 0; spin < SPIN_MAX; ++spin) {
      if (spin) __builtin_amdgcn_s_sleep(2);
      FRESH();
      const u32x2 f = LD64(WS_FL + (hg * 32 + ((lane + rb) & 31)) * 128);
      key = f[0] & 0x7ffffffu;
      const unsigned rdy = (unsigned)__ballot(f[1] == thi && (f[0] ^ tlo) < 0x8000000u);
      if (lane == 0) LDSU(L_CTL) = rdy;
      if (rdy == ~0u) break;
    }
    if (lane == 0) LDSU(L_CTL) = ~0u;
    ST(3);
    const unsigned best = wave_max_u32(key);
    const int sel = 2047 - (int)(best & 2047u);
    if (lane == 0) LDSU(L_CTL + 12) = best | 0x80000000u;
    if (has1) {
      const rsrc_t rg = mkrs(wg, D_ * F_ * 2), ru = mkrs(wu, D_ * F_ * 2);
      const unsigned vo = (lane >> 1) * (F_ * 2) + cg1 * 16;
#pragma unroll 1
      for (int i = 0; i < 16; ++i)
        asm volatile("s_mov_b32 m0, %0\n\ts_mov_b64 exec, %5\n\tbuffer_load_dwordx4 %1, %2, %4 offen lds\n\ts_not_b64 exec, exec\n\t"
                     "buffer_load_dwordx4 %1, %3, %4 offen lds\n\ts_mov_b64 exec, -1"
                     :: "s"(L_GU + i * 1024), "v"(vo), "s"(rg), "s"(ru), "s"(i * 32 * F_ * 2), "s"(0x5555555555555555ull) : "memory", "scc");
    }
    {
      const rsrc_t rd = mkrs(wd, D_ * F_ * 2);
      const unsigned vo = (ks2 * 224 + (lane >> 1)) * (D_ * 2) + cg2 * 32 + (lane & 1) * 16;
#pragma unroll 1
      for (int i = 0; i < 7; ++i) dma<L_WD>(vo, rd, i * 32 * D_ * 2, i * 1024);
    }
    if (has1) {
      dma<L_XS>(lane * 16, mkrs(x, 2048 * D_ * 2), sel * (D_ * 2), 0);
      ST(4);
      DRAIN();
      ST(5);
      f32x4 d = {0};
      const unsigned ba = l0 + L_GU + (8 * (lane >> 4) + q4) * 32 + p4 * 8;
#pragma unroll 4
      for (int u = 0; u < 16; ++u)
        d = MFMA16(lds_b128(l0 + L_XS + (4 * u + (lane >> 4)) * 16), cat8(lds_tr(ba + u * 1024), lds_tr(ba + u * 1024 + 128)), d);
      const float gp = bfround(d[0]), up = bfround(__shfl_down(d[0], 8));
      const unsigned hb = cvtpk(bfround(gp * sigmoidf_(gp)) * up, 0.f) & 0xffffu;
      if (lane < 8) ST64((u32x2{(tlo & 0xffff0000u) | hb, thi}), WS_H + (cg1 * 8 + lane) * 8);
    }
    ST(6);
#pragma unroll 1
    for (int i = 25; i < 80; ++i) dma<0>(vwo, ro, i * 4096, i * 1024);
    ST(7);
    for (int spin = 0; spin < SPIN_MAX; ++spin) {
      if (spin) __builtin_amdgcn_s_sleep(4);
      FRESH();
      const unsigned o = WS_H + ks2 * 1792 + (lane < 56 ? lane : 0) * 32;
      const u32x4 a = LD128(o), c = LD128(o + 16);
      const bool ok = a[1] == thi && a[3] == thi && c[1] == thi && c[3] == thi &&
                      ((a[0] ^ tlo) | (a[2] ^ tlo) | (c[0] ^ tlo) | (c[2] ^ tlo)) < 0x10000u;
      *(LAS u32x2*)(size_t)(l0 + L_HS + lane * 8) = u32x2{(a[0] & 0xffffu) | (a[2] << 16), (c[0] & 0xffffu) | (c[2] << 16)};
      if (__all(ok)) break;
    }
    ST(8);
    {
      f32x4 d = {0};
      const unsigned ba = l0 + L_WD + (8 * (lane >> 4) + q4) * 32 + p4 * 8;
#pragma unroll
      for (int u = 0; u < 7; ++u)
        d = MFMA16(lds_b128(l0 + L_HS + (4 * u + (lane >> 4)) * 16), cat8(lds_tr(ba + u * 1024), lds_tr(ba + u * 1024 + 128)), d);
      const unsigned yb = __float_as_uint(d[0]);
      if (lane < 16) ST128((u32x4{(tlo & 0xffff0000u) | (yb & 0xffffu), thi, (tlo & 0xffff0000u) | (yb >> 16), thi}),
                           WS_Y + (ks2 * 512 + cg2 * 16 + lane) * 16);
      asm volatile("s_waitcnt lgkmcnt(0)" ::: "memory");
    }
    ST(9);
#pragma unroll 1
    for (int i = 80; i < 128 + 25; ++i) { const int r = i & 127; dma<0>(vwo, ro, r * 4096, r * 1024); }
    for (int spin = 0; spin < SPIN_MAX; ++spin) {
      if (spin) __builtin_amdgcn_s_sleep(4);
      FRESH();
      const u32x2 f = LD64(WS_OL + (rb * 8 + (lane & 7)) * 128);
      if (__all(f[0] == tlo && f[1] == thi)) break;
    }
    ST(1);
    WAIT_BAR(0);
    BAR();
    STDUMP();
    return;
  }

  const int rot = (rb + hg) & 7;
  const rsrc_t rx = mkrs(x, 2048 * D_ * 2), rw = mkrs(W, D_ * 1536 * 2);
  const unsigned mw = w * 1024;
  const unsigned vx = (rb * 64 + w * 8 + (lane >> 3)) * 1024 + (((lane & 7) ^ ((w * 4 + (lane >> 4)) & 7)) << 4);
  const unsigned vq = ((lane >> 3) + w * 8) * 3072 + hg * 128 + (((lane & 7) ^ (((lane >> 4) & 1) << 2)) << 4);
  const unsigned vkv = vq + (w < 4 ? 1024 : 2048 - 32 * 3072);
  if (w == 4) dma<L_WV>(((lane + 8 * rot) & 63) * 16, mkrs(wr, 1024), 0, 0);
  if (w == 5) dma<L_WV + 1024>(((lane + 8 * rot) & 63) * 16, mkrs(gw, 1024), 0, 0);
#define KBLK(g) ((unsigned)(((g) + rot) & 7))
#define ISSX(g) dma<L_X + (g) * 8192>(vx, rx, KBLK(g) * 128, mw)
#define ISSUE(r) do { if ((r) < 16) dma<L_W + (((r) + 4) % 10) * 8192>(vkv, rw, KBLK((r) >> 1) * 196608 + ((r) & 1) * 98304, mw); \
                      else dma<L_W + (((r) + 4) % 10) * 8192>(vq, rw, KBLK(r) * 196608, mw); } while (0)
#define ISSG(g) do { ISSX(g); ISSUE(2 * (g)); ISSUE(2 * (g) + 1); } while (0)
#define LEAD 4
  ISSG(0); ISSG(1); ISSG(2);
  if (LEAD == 4) ISSG(3);

  const int part = w >> 2, pair = w & 1, tile = (w >> 1) & 1;
  unsigned xa0 = L_X + (tile * 32 + c31) * 128 + ((hh ^ ((c31 >> 1) & 7)) << 4);
  unsigned xa1 = xa0 ^ 32, xa2 = xa0 ^ 64, xa3 = xa0 ^ 96;
  unsigned wq0 = L_W + (8 * hh + q4) * 128 + (((4 * pair + 2 * blk + (p4 >> 1)) ^ (((q4 >> 1) & 1) << 2)) << 4) + 8 * (p4 & 1);
  unsigned wa0 = wq0 + part * 4096, wa1 = wa0 + 65536;
  asm volatile("" : "+v"(xa0), "+v"(xa1), "+v"(xa2), "+v"(xa3), "+v"(wa0), "+v"(wa1), "+v"(wq0));
  f32x16 acc = {0};
  GET_TAG();
  {
    bf16x8 wf[2][4], xf[2][4];
#define WADDR(g, u) ((((2 * (g) + 4 + ((u) >> 1)) % 10) >= 8 ? wa1 - 65536 : wa0) + ((2 * (g) + 4 + ((u) >> 1)) % 10) * 8192 + ((u) & 1) * 2048)
#define RD1(g, u, xa) do { xf[(g) & 1][u] = lds_b128(xa + (g) * 8192); \
      wf[(g) & 1][u] = cat8(lds_tr(WADDR(g, u)), lds_tr(WADDR(g, u) + 512)); } while (0)
#define RD4(g) do { RD1(g, 0, xa0); RD1(g, 1, xa1); RD1(g, 2, xa2); RD1(g, 3, xa3); } while (0)
#define MM(V, g, u) do { if (V) acc = MFMA32(xf[(g) & 1][u], wf[(g) & 1][u], acc); else acc = MFMA32(wf[(g) & 1][u], xf[(g) & 1][u], acc); SBAR(); } while (0)
#define NXT(g, i) do { if ((g) + LEAD <= 7) { if ((i) == 0) ISSX(((g) + LEAD) & 7); else ISSUE(2 * ((g) + LEAD) + (i) - 1); } \
      if ((g) >= 4 && (i) > 0) ISSUE(2 * (g) + 7 + (i)); } while (0)
#define GROUP(V, g, N) do { WAIT_BAR(N); STG(4 + (g)); MM(V, (g) - 1, 0); RD1(g, 0, xa0); NXT(g, 0); SBAR(); MM(V, (g) - 1, 1); RD1(g, 1, xa1); NXT(g, 1); SBAR(); \
      MM(V, (g) - 1, 2); RD1(g, 2, xa2); NXT(g, 2); SBAR(); MM(V, (g) - 1, 3); RD1(g, 3, xa3); SBAR(); } while (0)
#define WB0 9
#define KVLOOP_(V, N) do { WAIT_BAR(N); ST(1); RD4(0); NXT(0, 0); NXT(0, 1); NXT(0, 2); SBAR(); GROUP(V, 1, N); GROUP(V, 2, N); GROUP(V, 3, N); GROUP(V, 4, N); \
      GROUP(V, 5, 8); GROUP(V, 6, 7); if (LEAD == 4) GROUP(V, 7, 6); else GROUP(V, 7, 5); MM(V, 7, 0); MM(V, 7, 1); MM(V, 7, 2); MM(V, 7, 3); } while (0)
#define KVLOOP__(V, N) KVLOOP_(V, N)
#define KVLOOP(V) KVLOOP__(V, WB0)
    if (part == 0) KVLOOP(false); else KVLOOP(true);
  }
  ST(2);
  const u32x4 p0 = pack8(acc, 0), p1 = pack8(acc, 1);
  unsigned o0, o1;
  int h0, h1;
  if (part == 0) {
    const int s23 = (c31 & 19) | ((c31 & 4) << 1) | ((c31 & 8) >> 1);
    o0 = o1 = (s23 + 32 * hh) * 16; h0 = 2 * pair; h1 = 2 * pair + 1;
  } else {
    o0 = 1024 + (hh * 16 + (lane & 15)) * 16; o1 = o0 + 512; h0 = h1 = 2 * pair + blk;
  }
  ST128(p0, WS_KV + (((4 * hg + h0) * 2 + tile) * 32 + rb) * KVB + o0);
  ST128(p1, WS_KV + (((4 * hg + h1) * 2 + tile) * 32 + rb) * KVB + o1);
  if (part == 1 && lane < 2) ST128((u32x4{0x3F803F80u, 0x3F803F80u, 0x3F803F80u, 0x3F803F80u}), WS_KV + (((4 * hg + 2 * pair + lane) * 2 + tile) * 32 + rb) * KVB + 2048);
  if (part == 0) WAIT_BAR(2); else WAIT_BAR(3);
  ST(13);
  lds_w128(l0 + L_KV + (h0 * 2 + tile) * 2048 + o0, p0);
  lds_w128(l0 + L_KV + (h1 * 2 + tile) * 2048 + o1, p1);
  if (part == 0) {
    f32x16 qacc = {0};
    bf16x8 wf[2][4], xf[2][4];
#define QR1(r, u, xa) do { xf[(r) & 1][u] = lds_b128(xa + (r) * 8192); \
      wf[(r) & 1][u] = cat8(lds_tr(wq0 + (r) * 8192 + (u) * 2048), lds_tr(wq0 + (r) * 8192 + (u) * 2048 + 512)); } while (0)
#define QR4(r) do { QR1(r, 0, xa0); QR1(r, 1, xa1); QR1(r, 2, xa2); QR1(r, 3, xa3); } while (0)
    QR4(0);
    SBAR();
#define QS(r, u, xa) do { qacc = MFMA32(wf[(r) & 1][u], xf[(r) & 1][u], qacc); SBAR(); if ((r) < 7) QR1((r) + 1, u, xa); SBAR(); } while (0)
#define QG(r) do { QS(r, 0, xa0); QS(r, 1, xa1); QS(r, 2, xa2); QS(r, 3, xa3); } while (0)
    QG(0); QG(1); QG(2); QG(3); QG(4); QG(5); QG(6); QG(7);
    const float qs = 0.25f * 1.4426950408889634f;
    lds_w128(l0 + L_QX + ((2 * pair) * 2 + tile) * 1024 + lane * 16, pack8(qacc, 0, qs));
    lds_w128(l0 + L_QX + ((2 * pair + 1) * 2 + tile) * 1024 + lane * 16, pack8(qacc, 1, qs));
  } else {
    const int tok = 16 * (w & 3) + (lane & 15), g4 = lane >> 4;
    f32x4 d = {0};
    unsigned fa0 = L_WV + (lane & 1) * 1024 + g4 * 16, fb0 = L_X + tok * 128 + ((g4 ^ ((tok >> 1) & 7)) << 4), fb1 = fb0 ^ 64;
    asm volatile("" : "+v"(fa0), "+v"(fb0), "+v"(fb1));
    bf16x8 fa[16], fb[16];
#define RGR(u) do { fa[u] = lds_b128(fa0 + (u) * 64); fb[u] = lds_b128((((u) & 1) ? fb1 : fb0) + ((u) >> 1) * 8192); } while (0)
    RGR(0); RGR(1); RGR(2); RGR(3);
    SBAR();
#pragma unroll
    for (int u = 0; u < 16; ++u) { d = MFMA16(fa[u], fb[u], d); SBAR(); if (u + 4 < 16) RGR((u + 4) & 15); SBAR(); }
    const unsigned r16 = cvtpk(d[0], 0.f) & 0xffffu;
    unsigned key = (((r16 & 0x8000u) ? (~r16 & 0xffffu) : (r16 | 0x8000u)) << 11) | (2047 - (rb * 64 + tok));
    if (lane < 16) {
      asm volatile("ds_max_u32 %0, %1" :: "v"(L_CTL + 8), "v"(key) : "memory");
      *(LAS float*)(size_t)(l0 + L_RG + tok * 4) = bfround(sigmoidf_(bfround(d[1])));
    }
    if (w == 7 && lane < 2) { const unsigned v = lane ? 0u : 0x3F803F80u; lds_w128(l0 + L_ONE + lane * 16, u32x4{v, v, v, v}); }
  }
  ST(3);
  BAR();
  DRAIN();
  if (lane == 0 && __hip_atomic_fetch_add((LAS u32*)(size_t)(L_CTL + 4), 1u, __ATOMIC_RELAXED, __HIP_MEMORY_SCOPE_WORKGROUP) == 7)
    ST64((u32x2{(tlo & 0xf8000000u) | LDSU(L_CTL + 8), thi}), WS_FL + (hg * 32 + rb) * 128);
  ST(14);

  const int j = w & 3, kh = w >> 2, head = 4 * hg + j;
  const bool onerow = c31 == 16 || c31 == 20;
  const unsigned idx = 0xDDDDDDDDu, vk = lane * 16;
  const unsigned vv0 = c31 < 16 ? 1024 + (hh * 16 + c31) * 16 : (onerow ? 2048u : 0x7f000000u);
  const unsigned vv1 = c31 < 16 ? vv0 + 512 : vv0;
  const bf16x8 qa = lds_b128(l0 + L_QX + (j * 2) * 1024 + lane * 16), qb = lds_b128(l0 + L_QX + (j * 2 + 1) * 1024 + lane * 16);
  const float gate = *(const LAS float*)(size_t)(l0 + L_RG + ((hg >> 2) * 32 + c31) * 4);
  f32x16 sa, sb, mm;
  {
    const unsigned lk = l0 + L_KV + (j * 2 + kh) * 2048;
    const unsigned la = lk + vk, lm = l0 + L_KV + j * 4096 + vk;
    const unsigned lv0 = c31 < 16 ? lk + vv0 : l0 + L_ONE + (onerow ? 0 : 16), lv1 = c31 < 16 ? lv0 + 512 : lv0;
#define Z1(r) "v_accvgpr_write_b32 a[" STR(r) "], 0\n\t"
#define Z4(r) Z1(r) Z1(r+1) Z1(r+2) Z1(r+3)
#define Z16(r) Z4(r) Z4(r+4) Z4(r+8) Z4(r+12)
    asm volatile("ds_read_b128 " KS(1) ", %[lm]\n\tds_read_b128 " KS(0) ", %[la]\n\tds_read_b128 " V0(0) ", %[a0]\n\tds_read_b128 " V1(0) ", %[a1]\n\t"
                 Z16(0) Z16(16)
                 "s_waitcnt lgkmcnt(0)\n\ts_barrier\n\t"
                 "v_mfma_f32_32x32x16_bf16 v[36:51], " KS(1) ", %[qa], 0\n\t"
                 "v_mfma_f32_32x32x16_bf16 v[52:67], " KS(1) ", %[qb], 0\n\t"
                 "s_nop 15\n\ts_nop 3\n\t"
                 : "=&{v[36:51]}"(sa), "=&{v[52:67]}"(sb)
                 : [lm] "v"(lm), [la] "v"(la), [a0] "v"(lv0), [a1] "v"(lv1), [qa] "v"(qa), [qb] "v"(qb)
                 : "memory", ACLOB);
    const float nm = -halves_max(fmaxf(max16(sa), max16(sb)));
#pragma unroll
    for (int i = 0; i < 16; ++i) mm[i] = nm;
    asm volatile("s_nop 1\n\t" QKA(0) QKB(0) "s_nop 11\n\t"
                 EX4(36) EX4(40) EX4(44) SMF(OA, V0(0), 36) EX4(48) EX4(52) SMF(OA, V1(0), 44) EX4(56) EX4(60) SMF(OB, V0(0), 52) EX4(64)
                 : "=&{v[36:51]}"(sa), "=&{v[52:67]}"(sb)
                 : "{v[68:83]}"(mm), [qa] "v"(qa), [qb] "v"(qb), [idx] "v"(idx) : "memory", ACLOB);
  }
  ST(4);
  ST(12);
  const unsigned kvb = WS_KV + (head * 2 + kh) * 32 * KVB;
  const unsigned tbl = kvb + ((lane + rb) & 31) * KVB;
  unsigned todo = 0xfffffffeu, ready = 0;
  int spins = 0;
  auto pick = [&]() -> unsigned {
    while (!(ready & todo)) {
      if (spins) __builtin_amdgcn_s_sleep(1);
      ready = __builtin_amdgcn_readfirstlane(LDSU(L_CTL));
      if (++spins > (SPIN_MAX << 4)) ready = ~0u;
    }
    const int i = __builtin_ctz(ready & todo);
    todo &= ~(1u << i);
    return __builtin_amdgcn_readlane(tbl, i);
  };
  unsigned s3, s4, s5, s6, s7;
  {
    const unsigned s1 = pick(), s2 = pick();
    s3 = pick(); s4 = pick();
    ST(5);
    asm volatile("s_nop 4\n\t" LDK(1, "%[s1]") LDV(1, "%[s1]") LDK(2, "%[s2]") LDV(2, "%[s2]") LDK(3, "%[s3]") LDV(3, "%[s3]") LDK(0, "%[s4]")
                 "s_waitcnt vmcnt(9)\n\t" QKA(1) "s_nop 15\n\t"
                 : "=&{v[36:51]}"(sa)
                 : "{v[68:83]}"(mm), [qa] "v"(qa), [vk] "v"(vk), [v0] "v"(vv0), [v1] "v"(vv1), [rs] "s"(rs),
                   [s1] "s"(s1), [s2] "s"(s2), [s3] "s"(s3), [s4] "s"(s4)
                 : "memory", ACLOB);
  }
  ST(6);
  __builtin_amdgcn_s_setprio(2);
  s5 = pick(); TILE(1, 0, 2, s4, s5);
  s6 = pick(); TILE(2, 1, 3, s5, s6);
  s7 = pick(); TILE(3, 2, 0, s6, s7);
#pragma unroll 1
  for (int t = 4; t < 28; t += 4) {
    if (t == 20) __builtin_amdgcn_s_setprio(1);
    s4 = pick(); TILE(0, 3, 1, s7, s4);
    s5 = pick(); TILE(1, 0, 2, s4, s5);
    s6 = pick(); TILE(2, 1, 3, s5, s6);
    s7 = pick(); TILE(3, 2, 0, s6, s7);
  }
  TILE(0, 3, 1, s7, s7);
  TILE(1, 0, 2, s7, s7);
  __builtin_amdgcn_s_setprio(0);
  TILE(2, 1, 3, s7, s7);
  TILE(3, 2, 0, s7, s7);
  float oa[9], ob[9];
#define RD(i, r) "v_accvgpr_read_b32 %" STR(i) ", a" STR(r) "\n\t"
  asm volatile("s_nop 1\n\t" SMF(OB, V1(3), 60) "s_waitcnt vmcnt(0)\n\ts_nop 15\n\ts_nop 3\n\t"
               RD(0, 0) RD(1, 1) RD(2, 2) RD(3, 3) RD(4, 4) RD(5, 5) RD(6, 6) RD(7, 7) RD(8, 8)
               RD(9, 16) RD(10, 17) RD(11, 18) RD(12, 19) RD(13, 20) RD(14, 21) RD(15, 22) RD(16, 23) RD(17, 24)
               : "=v"(oa[0]), "=v"(oa[1]), "=v"(oa[2]), "=v"(oa[3]), "=v"(oa[4]), "=v"(oa[5]), "=v"(oa[6]), "=v"(oa[7]), "=v"(oa[8]),
                 "=v"(ob[0]), "=v"(ob[1]), "=v"(ob[2]), "=v"(ob[3]), "=v"(ob[4]), "=v"(ob[5]), "=v"(ob[6]), "=v"(ob[7]), "=v"(ob[8])
               : "{v[52:67]}"(sb), [idx] "v"(idx) : "memory", ACLOB);
  ST(7);
  TAIL(ws, x, out, rs, b, w, oa, ob, qa, qb, vk, vv0, vv1, idx, gate, tlo, thi STPASS);
  STDUMP();
}

extern "C" void kernel_launch(void* const* d_in, const int* in_sizes, int n_in, void* d_out, int out_size, void* d_ws, size_t ws_size,
                              hipStream_t stream) {
  (void)hipFuncSetAttribute((const void*)fused, hipFuncAttributeMaxDynamicSharedMemorySize, LDS_BYTES);
  fused<<<256, 576, LDS_BYTES, stream>>>((const u16*)d_in[0], (const u16*)d_in[1], (const u16*)d_in[2], (const u16*)d_in[4],
                                         (const u16*)d_in[5], (const u16*)d_in[6], (const u16*)d_in[7], (const u16*)d_in[8],
                                         (char*)d_ws, (u16*)d_out);
}
```

```cpp
#define PFCODE
#define PFPAGE
#include <hip/hip_runtime.h>
#include <hip/hip_bf16.h>

typedef unsigned short u16;
typedef unsigned int u32;
typedef unsigned long long u64;
typedef __attribute__((ext_vector_type(8))) __bf16 bf16x8;
typedef __attribute__((ext_vector_type(16))) __bf16 bf16x16;
typedef __attribute__((ext_vector_type(16))) float f32x16;
typedef __attribute__((ext_vector_type(8))) float f32x8;
typedef __attribute__((ext_vector_type(4))) float f32x4;
typedef __attribute__((ext_vector_type(4))) unsigned u32x4;
typedef __attribute__((ext_vector_type(2))) unsigned u32x2;
typedef __attribute__((ext_vector_type(4))) short s16x4;
#define LAS __attribute__((address_space(3)))

constexpr int D_ = 512, F_ = 1792;
constexpr u32 KVB = 2176;
constexpr u32 WS_KV = 0;
constexpr u32 WS_OF = 8u << 20;
constexpr u32 WS_FL = 10u << 20;
constexpr u32 WS_OL = WS_FL + 32768;
constexpr u32 WS_H  = WS_OL + 32768;
constexpr u32 WS_Y  = WS_H + 16384;
constexpr u32 WS_EP = WS_Y + 65536;
constexpr u32 WS_END = WS_EP + 128;
constexpr u32 L_X = 0, L_W = 65536, L_KV = 131072, L_WV = 147456, L_QX = 149504, L_RG = 157696, L_ONE = 158208;
constexpr u32 L_GU = 0, L_WD = 16384, L_XS = 23552, L_HS = 24576;
constexpr u32 L_OS = 131072;
constexpr u32 L_CTL = 163808;
constexpr int LDS_BYTES = 163840;
constexpr int SPIN_MAX = 1 << 18;

#define STG(i)
#define ST(i) ST_(i)
#define ST_(i)
#define STDECL
#define STDUMP()
#define STARG
#define STPASS

#define HPRIO 3
#define MAP 3
#define MAPB(b, hg, rb) do { if (MAP == 3) { hg = 2 * (b & 3) + ((b >> 3) & 1); rb = 16 * ((b >> 2) & 1) + (b >> 4); } \
  else if (MAP == 2) { hg = 4 * (b & 1) + ((b >> 3) & 3); rb = 8 * ((b >> 1) & 3) + (b >> 5); } \
  else if (MAP == 1) { hg = b & 7; rb = b >> 3; } else { hg = (b >> 3) & 7; rb = 4 * (b & 7) + (b >> 6); } } while (0)
typedef __amdgpu_buffer_rsrc_t rsrc_t;
__device__ __forceinline__ rsrc_t mkrs(const void* p, unsigned bytes) { return __builtin_amdgcn_make_buffer_rsrc((void*)p, 0, (int)bytes, 0x00020000); }
template <unsigned ADD>
__device__ __forceinline__ void dma(unsigned voff, rsrc_t r, unsigned soff, unsigned m) {
  asm volatile("s_add_u32 m0, %0, %c4\n\ts_nop 0\n\tbuffer_load_dwordx4 %1, %2, %3 offen lds" :: "s"(m), "v"(voff), "s"(r), "s"(soff), "i"(ADD) : "memory", "scc");
}
__device__ __forceinline__ unsigned cvtpk(float lo, float hi) {
  typedef __attribute__((ext_vector_type(2))) __bf16 bf16x2;
  typedef __attribute__((ext_vector_type(2))) float f32x2;
  f32x2 v = {lo, hi};
  return __builtin_bit_cast(unsigned, __builtin_convertvector(v, bf16x2));
}
__device__ __forceinline__ float bfround(float f) { return __uint_as_float(cvtpk(f, 0.f) << 16); }
__device__ __forceinline__ float bflo(unsigned u) { return __uint_as_float(u << 16); }
__device__ __forceinline__ float bfhi(unsigned u) { return __uint_as_float(u & 0xffff0000u); }
__device__ __forceinline__ float sigmoidf_(float z) { return __builtin_amdgcn_rcpf(1.f + __expf(-z)); }
__device__ __forceinline__ bf16x8 lds_b128(unsigned a) { return *(const LAS bf16x8*)(size_t)a; }
__device__ __forceinline__ void lds_w128(unsigned a, u32x4 v) { *(LAS u32x4*)(size_t)a = v; }
__device__ __forceinline__ s16x4 lds_tr(unsigned a) { return __builtin_amdgcn_ds_read_tr16_b64_v4i16((LAS s16x4*)(size_t)a); }
__device__ __forceinline__ bf16x8 cat8(s16x4 a, s16x4 b) {
  typedef __attribute__((ext_vector_type(8))) short s16x8;
  s16x8 r = {a[0], a[1], a[2], a[3], b[0], b[1], b[2], b[3]};
  return __builtin_bit_cast(bf16x8, r);
}
__device__ __forceinline__ u32x4 pack8(const f32x16& a, int z, float s) {
  return u32x4{cvtpk(a[8 * z] * s, a[8 * z + 1] * s), cvtpk(a[8 * z + 2] * s, a[8 * z + 3] * s),
               cvtpk(a[8 * z + 4] * s, a[8 * z + 5] * s), cvtpk(a[8 * z + 6] * s, a[8 * z + 7] * s)};
}
__device__ __forceinline__ u32x4 pack8(const f32x16& a, int z) {
  return u32x4{cvtpk(a[8 * z], a[8 * z + 1]), cvtpk(a[8 * z + 2], a[8 * z + 3]), cvtpk(a[8 * z + 4], a[8 * z + 5]), cvtpk(a[8 * z + 6], a[8 * z + 7])};
}
__device__ __forceinline__ unsigned wave_max_u32(unsigned v) {
#pragma unroll
  for (int m = 32; m >= 1; m >>= 1) v = max(v, (unsigned)__shfl_xor((int)v, m));
  return __builtin_amdgcn_readfirstlane(v);
}
#define MFMA32(a, b, c) __builtin_amdgcn_mfma_f32_32x32x16_bf16(a, b, c, 0, 0, 0)
#define MFMA16(a, b, c) __builtin_amdgcn_mfma_f32_16x16x32_bf16(a, b, c, 0, 0, 0)
#define WAIT_BAR(N) asm volatile("s_waitcnt vmcnt(" #N ") lgkmcnt(0)\n\ts_barrier" ::: "memory")
#define BAR() asm volatile("s_waitcnt lgkmcnt(0)\n\ts_barrier" ::: "memory")
#define DRAIN() asm volatile("s_waitcnt vmcnt(0)" ::: "memory")
#define SBAR() __builtin_amdgcn_sched_barrier(0)
#define FRESH() asm volatile("" ::: "memory")
#define LDSU(a) (*(volatile LAS u32*)(size_t)(a))
#define LD128(off) __builtin_amdgcn_raw_buffer_load_b128(rs, (off), 0, 16)
#define LD64(off) __builtin_amdgcn_raw_buffer_load_b64(rs, (off), 0, 16)
#define ST128(v, off) __builtin_amdgcn_raw_buffer_store_b128((v), rs, (off), 0, 16)
#define ST64(v, off) __builtin_amdgcn_raw_buffer_store_b64((v), rs, (off), 0, 16)

#define STR_(x) #x
#define STR(x) STR_(x)
#define EX1(b) "v_exp_f32 v[" STR(b) "], v[" STR(b) "]\n\t"
#define EX4(b) EX1(b) EX1(b+1) EX1(b+2) EX1(b+3)
#define AR4(b) "a[" STR(b) ":" STR(b) "+3]"
#define KS(s) AR4(32+4*s)
#define V0(s) AR4(48+8*s)
#define V1(s) AR4(52+8*s)
#define SMF(acc, va, sb) "v_smfmac_f32_32x32x32_bf16 " acc ", " va ", v[" STR(sb) ":" STR(sb) "+7], %[idx]\n\t"
#define OA "a[0:15]"
#define OB "a[16:31]"
#define QKA(s) "v_mfma_f32_32x32x16_bf16 v[36:51], " KS(s) ", %[qa], v[68:83]\n\t"
#define QKB(s) "v_mfma_f32_32x32x16_bf16 v[52:67], " KS(s) ", %[qb], v[68:83]\n\t"
#define LDK(s, so) "buffer_load_dwordx4 " KS(s) ", %[vk], %[rs], " so " offen sc1\n\t"
#define LDV(s, so) "buffer_load_dwordx4 " V0(s) ", %[v0], %[rs], " so " offen sc1\n\t" \
                   "buffer_load_dwordx4 " V1(s) ", %[v1], %[rs], " so " offen sc1\n\t"
#define ACLOB "a0","a1","a2","a3","a4","a5","a6","a7","a8","a9","a10","a11","a12","a13","a14","a15","a16","a17","a18","a19", \
  "a20","a21","a22","a23","a24","a25","a26","a27","a28","a29","a30","a31","a32","a33","a34","a35","a36","a37","a38","a39", \
  "a40","a41","a42","a43","a44","a45","a46","a47","a48","a49","a50","a51","a52","a53","a54","a55","a56","a57","a58","a59", \
  "a60","a61","a62","a63","a64","a65","a66","a67","a68","a69","a70","a71","a72","a73","a74","a75","a76","a77","a78","a79"
#define TILE(s, sp, sn, soV, soK) \
  asm volatile(EX4(36) SMF(OB, V1(sp), 60) LDV(sp, "%[sv]") EX4(40) QKB(s) LDK(s, "%[sk]") EX4(44) \
               "s_waitcnt vmcnt(9)\n\t" SMF(OA, V0(s), 36) EX4(48) \
               EX4(52) SMF(OA, V1(s), 44) EX4(56) QKA(sn) EX4(60) SMF(OB, V0(s), 52) EX4(64) \
               : "+{v[36:51]}"(sa), "+{v[52:67]}"(sb) \
               : "{v[68:83]}"(mm), [qa] "v"(qa), [qb] "v"(qb), [idx] "v"(idx), [vk] "v"(vk), [v0] "v"(vv0), [v1] "v"(vv1), [rs] "s"(rs), \
                 [sv] "s"(soV), [sk] "s"(soK) : "memory", ACLOB)

__device__ __forceinline__ float max16(const f32x16& p) {
  float a = fmaxf(fmaxf(p[0], p[1]), p[2]), b = fmaxf(fmaxf(p[3], p[4]), p[5]);
  a = fmaxf(fmaxf(a, p[6]), p[7]); b = fmaxf(fmaxf(b, p[8]), p[9]);
  a = fmaxf(fmaxf(a, p[10]), p[11]); b = fmaxf(fmaxf(b, p[12]), p[13]);
  a = fmaxf(fmaxf(a, p[14]), p[15]);
  return fmaxf(a, b);
}
__device__ __forceinline__ float halves_max(float m) {
  auto rr = __builtin_amdgcn_permlane32_swap(__float_as_uint(m), __float_as_uint(m), false, false);
  return fmaxf(__uint_as_float(rr[0]), __uint_as_float(rr[1]));
}

__device__ __forceinline__ void TAIL(char* ws, const u16* x, u16* out, __amdgpu_buffer_rsrc_t rs, int b, int w,
    const float (&oa)[9], const float (&ob)[9], bf16x8 qa, bf16x8 qb, unsigned vk, unsigned vv0, unsigned vv1, unsigned idx,
    float gate, u32 tlo, u32 thi STARG) {
  constexpr unsigned l0 = 0;
  asm volatile("" : "+v"(vk));
  const int lane = vk >> 4;
  int hg, rb; MAPB(b, hg, rb);
  const int c31 = lane & 31, hh = lane >> 5, blk = (lane >> 4) & 1, q4 = (lane & 15) >> 2, p4 = lane & 3;
  const int tt = 2 * rb + (hg >> 2), cg = hg & 3, j = w & 3, kh = w >> 2, head = 4 * hg + j;
#pragma unroll
  for (int i = 0; i < 9; ++i) *(LAS float*)(size_t)(l0 + L_OS + (w ^ 4) * 4096 + i * 256 + lane * 4) = kh ? oa[i] : ob[i];
  unsigned best = 0;
  for (int spin = 0; spin < (SPIN_MAX << 4) && !best; ++spin) best = __builtin_amdgcn_readfirstlane(LDSU(L_CTL + 12));
  best &= 0x7fffffffu;
  BAR();
  float o[9];
  bool bad = false;
#pragma unroll
  for (int i = 0; i < 9; ++i) {
    o[i] = (kh ? ob[i] : oa[i]) + *(const LAS float*)(size_t)(l0 + L_OS + w * 4096 + i * 256 + lane * 4);
    bad |= !(fabsf(o[i]) < 1e30f);
  }
  bad |= !(o[8] > 1e-18f && o[8] < 1e18f);
  if (__any(bad)) {
    const bf16x8 q = kh ? qb : qa;
    const unsigned kb0 = WS_KV + head * 64 * KVB;
    float m = -3.0e38f;
#pragma unroll 1
    for (int t = 0; t < 64; ++t) {
      const f32x16 s = MFMA32(__builtin_bit_cast(bf16x8, LD128(kb0 + t * KVB + vk)), q, (f32x16{0}));
      m = fmaxf(m, max16(s));
    }
    m = -halves_max(m);
    f32x16 c, oc = {0};
#pragma unroll
    for (int i = 0; i < 16; ++i) c[i] = m;
#pragma unroll 1
    for (int t = 0; t < 64; ++t) {
      const f32x16 s = MFMA32(__builtin_bit_cast(bf16x8, LD128(kb0 + t * KVB + vk)), q, c);
      f32x8 e0, e1;
#pragma unroll
      for (int i = 0; i < 8; ++i) { e0[i] = __builtin_amdgcn_exp2f(s[i]); e1[i] = __builtin_amdgcn_exp2f(s[8 + i]); }
      const u32x4 va = __builtin_amdgcn_raw_buffer_load_b128(rs, vv0, kb0 + t * KVB, 16);
      const u32x4 vb = __builtin_amdgcn_raw_buffer_load_b128(rs, vv1, kb0 + t * KVB, 16);
      oc = __builtin_amdgcn_smfmac_f32_32x32x32_bf16(__builtin_bit_cast(bf16x8, va), __builtin_bit_cast(bf16x16, e0), oc, (int)idx, 0, 0);
      oc = __builtin_amdgcn_smfmac_f32_32x32x32_bf16(__builtin_bit_cast(bf16x8, vb), __builtin_bit_cast(bf16x16, e1), oc, (int)idx, 0, 0);
    }
#pragma unroll
    for (int i = 0; i < 9; ++i) o[i] = oc[i];
  }
  {
    const float rl = 1.f / o[8];
    const unsigned lim = 0xfffffffeu;
    ST128((u32x4{min(cvtpk(o[0] * rl, o[1] * rl), lim), min(cvtpk(o[2] * rl, o[3] * rl), lim), min(cvtpk(o[4] * rl, o[5] * rl), lim),
                 min(cvtpk(o[6] * rl, o[7] * rl), lim)}), WS_OF + ((2 * rb + kh) * 32 + head) * 1024 + vk);
  }
  ST(8);

  const int token = tt * 32 + c31, col = cg * 128 + (w & 3) * 32 + hh * 16;
  const int sel = 2047 - (int)(best & 2047u);
  u32x4 xa, xc;
  float yv = 0.f;
  const unsigned abase = (4 * hh + q4) * 256 + (((4 * (w & 3) + 2 * (p4 & 1) + blk) ^ (q4 << 2)) << 4) + 8 * (p4 >> 1);
  if (w < 4) {
    asm volatile(
      "ds_read_b64_tr_b16 a[0:1], %0 offset:0\n\t"
      "ds_read_b64_tr_b16 a[2:3], %0 offset:2048\n\t"
      "ds_read_b64_tr_b16 a[4:5], %0 offset:4096\n\t"
      "ds_read_b64_tr_b16 a[6:7], %0 offset:6144\n\t"
      "ds_read_b64_tr_b16 a[8:9], %0 offset:8192\n\t"
      "ds_read_b64_tr_b16 a[10:11], %0 offset:10240\n\t"
      "ds_read_b64_tr_b16 a[12:13], %0 offset:12288\n\t"
      "ds_read_b64_tr_b16 a[14:15], %0 offset:14336\n\t"
      "ds_read_b64_tr_b16 a[16:17], %0 offset:16384\n\t"
      "ds_read_b64_tr_b16 a[18:19], %0 offset:18432\n\t"
      "ds_read_b64_tr_b16 a[20:21], %0 offset:20480\n\t"
      "ds_read_b64_tr_b16 a[22:23], %0 offset:22528\n\t"
      "ds_read_b64_tr_b16 a[24:25], %0 offset:24576\n\t"
      "ds_read_b64_tr_b16 a[26:27], %0 offset:26624\n\t"
      "ds_read_b64_tr_b16 a[28:29], %0 offset:28672\n\t"
      "ds_read_b64_tr_b16 a[30:31], %0 offset:30720\n\t"
      "ds_read_b64_tr_b16 a[32:33], %0 offset:32768\n\t"
      "ds_read_b64_tr_b16 a[34:35], %0 offset:34816\n\t"
      "ds_read_b64_tr_b16 a[36:37], %0 offset:36864\n\t"
      "ds_read_b64_tr_b16 a[38:39], %0 offset:38912\n\t"
      "ds_read_b64_tr_b16 a[40:41], %0 offset:40960\n\t"
      "ds_read_b64_tr_b16 a[42:43], %0 offset:43008\n\t"
      "ds_read_b64_tr_b16 a[44:45], %0 offset:45056\n\t"
      "ds_read_b64_tr_b16 a[46:47], %0 offset:47104\n\t"
      "ds_read_b64_tr_b16 a[48:49], %0 offset:49152\n\t"
      "ds_read_b64_tr_b16 a[50:51], %0 offset:51200\n\t"
      "ds_read_b64_tr_b16 a[52:53], %0 offset:53248\n\t"
      "ds_read_b64_tr_b16 a[54:55], %0 offset:55296\n\t"
      "ds_read_b64_tr_b16 a[56:57], %0 offset:57344\n\t"
      "ds_read_b64_tr_b16 a[58:59], %0 offset:59392\n\t"
      "ds_read_b64_tr_b16 a[60:61], %0 offset:61440\n\t"
      "ds_read_b64_tr_b16 a[62:63], %0 offset:63488\n\t"
      "ds_read_b64_tr_b16 a[64:65], %1 offset:0\n\t"
      "ds_read_b64_tr_b16 a[66:67], %1 offset:2048\n\t"
      "ds_read_b64_tr_b16 a[68:69], %1 offset:4096\n\t"
      "ds_read_b64_tr_b16 a[70:71], %1 offset:6144\n\t"
      "ds_read_b64_tr_b16 a[72:73], %1 offset:8192\n\t"
      "ds_read_b64_tr_b16 a[74:75], %1 offset:10240\n\t"
      "ds_read_b64_tr_b16 a[76:77], %1 offset:12288\n\t"
      "ds_read_b64_tr_b16 a[78:79], %1 offset:14336\n\t"
      :: "v"(abase), "v"(abase + 65536) : "memory", ACLOB);
    xa = *(const u32x4*)(x + (size_t)token * D_ + col); xc = *(const u32x4*)(x + (size_t)token * D_ + col + 8);
    if ((sel >> 5) == tt) {
      for (int spin = 0; spin < SPIN_MAX; ++spin) {
      if (spin) __builtin_amdgcn_s_sleep(4);
      FRESH();
        bool ok = true; yv = 0.f;
#pragma unroll
        for (int i = 0; i < 4; ++i) {
          const u32x4 a = LD128(WS_Y + ((4 * hh + i) * 512 + cg * 128 + w * 32 + c31) * 16);
          ok &= a[1] == thi && a[3] == thi && ((a[0] ^ tlo) | (a[2] ^ tlo)) < 0x10000u;
          yv += __uint_as_float((a[0] & 0xffffu) | (a[2] << 16));
        }
        if (__all(ok)) break;
      }
      yv += __shfl_xor(yv, 32);
    }
  }
  float tb[16];
  if (w < 4) {
#pragma unroll
    for (int i = 0; i < 4; ++i) { tb[2 * i] = bflo(xa[i]); tb[2 * i + 1] = bfhi(xa[i]); tb[8 + 2 * i] = bflo(xc[i]); tb[9 + 2 * i] = bfhi(xc[i]); }
    if ((sel >> 5) == tt) {
      const unsigned srt = best >> 11;
      const float rsel = bfround(sigmoidf_(bflo((srt & 0x8000u) ? (srt & 0x7fffu) : (~srt & 0xffffu))));
      const float yy = bfround(bfround(yv) * rsel);
#pragma unroll
      for (int i = 0; i < 16; ++i) { const float t = __shfl(yy, hh * 16 + i); if (token == sel) tb[i] = bfround(tb[i] + t); }
    }
    const float og = bfround(1.f - gate);
#pragma unroll
    for (int i = 0; i < 16; ++i) tb[i] *= og;
  }
  {
    u32x4 v[4];
    for (int spin = 0; spin < SPIN_MAX; ++spin) {
      if (spin) __builtin_amdgcn_s_sleep(4);
      FRESH();
      unsigned m = 0;
#pragma unroll
      for (int i = 0; i < 4; ++i) v[i] = LD128(WS_OF + (tt * 32 + 4 * w + i) * 1024 + vk);
#pragma unroll
      for (int i = 0; i < 4; ++i) m = max(max(m, max(v[i][0], v[i][1])), max(v[i][2], v[i][3]));
      if (!__any(m == ~0u)) break;
    }
    ST(9);
#pragma unroll
    for (int i = 0; i < 4; ++i) lds_w128(l0 + L_OS + (4 * w + i) * 1024 + vk, v[i]);
  }
  BAR();
  ST(10);
  if (w < 4) {
    f32x16 a;
    asm volatile(
      "ds_read_b128 v[20:23], %[b] offset:0\n\t"
      "ds_read_b128 v[24:27], %[b] offset:1024\n\t"
      "ds_read_b128 v[28:31], %[b] offset:2048\n\t"
      "ds_read_b128 v[32:35], %[b] offset:3072\n\t"
      "ds_read_b128 v[36:39], %[b] offset:4096\n\t"
      "ds_read_b128 v[40:43], %[b] offset:5120\n\t"
      "ds_read_b128 v[44:47], %[b] offset:6144\n\t"
      "ds_read_b128 v[48:51], %[b] offset:7168\n\t"
      "s_waitcnt lgkmcnt(7)\n\t"
      "v_mfma_f32_32x32x16_bf16 v[68:83], a[0:3], v[20:23], 0\n\t"
      "ds_read_b128 v[20:23], %[b] offset:8192\n\t"
      "ds_read_b64_tr_b16 a[0:1], %[a1] offset:16384\n\t"
      "ds_read_b64_tr_b16 a[2:3], %[a1] offset:18432\n\t"
      "s_waitcnt lgkmcnt(9)\n\t"
      "v_mfma_f32_32x32x16_bf16 v[68:83], a[4:7], v[24:27], v[68:83]\n\t"
      "ds_read_b128 v[24:27], %[b] offset:9216\n\t"
      "ds_read_b64_tr_b16 a[4:5], %[a1] offset:20480\n\t"
      "ds_read_b64_tr_b16 a[6:7], %[a1] offset:22528\n\t"
      "s_waitcnt lgkmcnt(11)\n\t"
      "v_mfma_f32_32x32x16_bf16 v[68:83], a[8:11], v[28:31], v[68:83]\n\t"
      "ds_read_b128 v[28:31], %[b] offset:10240\n\t"
      "ds_read_b64_tr_b16 a[8:9], %[a1] offset:24576\n\t"
      "ds_read_b64_tr_b16 a[10:11], %[a1] offset:26624\n\t"
      "s_waitcnt lgkmcnt(13)\n\t"
      "v_mfma_f32_32x32x16_bf16 v[68:83], a[12:15], v[32:35], v[68:83]\n\t"
      "ds_read_b128 v[32:35], %[b] offset:11264\n\t"
      "ds_read_b64_tr_b16 a[12:13], %[a1] offset:28672\n\t"
      "ds_read_b64_tr_b16 a[14:15], %[a1] offset:30720\n\t"
      "s_waitcnt lgkmcnt(15)\n\t"
      "v_mfma_f32_32x32x16_bf16 v[68:83], a[16:19], v[36:39], v[68:83]\n\t"
      "ds_read_b128 v[36:39], %[b] offset:12288\n\t"
      "ds_read_b64_tr_b16 a[16:17], %[a1] offset:32768\n\t"
      "ds_read_b64_tr_b16 a[18:19], %[a1] offset:34816\n\t"
      "s_waitcnt lgkmcnt(15)\n\t"
      "v_mfma_f32_32x32x16_bf16 v[68:83], a[20:23], v[40:43], v[68:83]\n\t"
      "ds_read_b128 v[40:43], %[b] offset:13312\n\t"
      "ds_read_b64_tr_b16 a[20:21], %[a1] offset:36864\n\t"
      "ds_read_b64_tr_b16 a[22:23], %[a1] offset:38912\n\t"
      "s_waitcnt lgkmcnt(15)\n\t"
      "v_mfma_f32_32x32x16_bf16 v[68:83], a[24:27], v[44:47], v[68:83]\n\t"
      "ds_read_b128 v[44:47], %[b] offset:14336\n\t"
      "ds_read_b64_tr_b16 a[24:25], %[a1] offset:40960\n\t"
      "ds_read_b64_tr_b16 a[26:27], %[a1] offset:43008\n\t"
      "s_waitcnt lgkmcnt(15)\n\t"
      "v_mfma_f32_32x32x16_bf16 v[68:83], a[28:31], v[48:51], v[68:83]\n\t"
      "ds_read_b128 v[48:51], %[b] offset:15360\n\t"
      "ds_read_b64_tr_b16 a[28:29], %[a1] offset:45056\n\t"
      "ds_read_b64_tr_b16 a[30:31], %[a1] offset:47104\n\t"
      "s_waitcnt lgkmcnt(15)\n\t"
      "v_mfma_f32_32x32x16_bf16 v[68:83], a[32:35], v[20:23], v[68:83]\n\t"
      "ds_read_b128 v[20:23], %[b] offset:16384\n\t"
      "ds_read_b64_tr_b16 a[32:33], %[a1] offset:49152\n\t"
      "ds_read_b64_tr_b16 a[34:35], %[a1] offset:51200\n\t"
      "s_waitcnt lgkmcnt(15)\n\t"
      "v_mfma_f32_32x32x16_bf16 v[68:83], a[36:39], v[24:27], v[68:83]\n\t"
      "ds_read_b128 v[24:27], %[b] offset:17408\n\t"
      "ds_read_b64_tr_b16 a[36:37], %[a1] offset:53248\n\t"
      "ds_read_b64_tr_b16 a[38:39], %[a1] offset:55296\n\t"
      "s_waitcnt lgkmcnt(15)\n\t"
      "v_mfma_f32_32x32x16_bf16 v[68:83], a[40:43], v[28:31], v[68:83]\n\t"
      "ds_read_b128 v[28:31], %[b] offset:18432\n\t"
      "ds_read_b64_tr_b16 a[40:41], %[a1] offset:57344\n\t"
      "ds_read_b64_tr_b16 a[42:43], %[a1] offset:59392\n\t"
      "s_waitcnt lgkmcnt(15)\n\t"
      "v_mfma_f32_32x32x16_bf16 v[68:83], a[44:47], v[32:35], v[68:83]\n\t"
      "ds_read_b128 v[32:35], %[b] offset:19456\n\t"
      "ds_read_b64_tr_b16 a[44:45], %[a1] offset:61440\n\t"
      "ds_read_b64_tr_b16 a[46:47], %[a1] offset:63488\n\t"
      "s_waitcnt lgkmcnt(15)\n\t"
      "v_mfma_f32_32x32x16_bf16 v[68:83], a[48:51], v[36:39], v[68:83]\n\t"
      "ds_read_b128 v[36:39], %[b] offset:20480\n\t"
      "s_waitcnt lgkmcnt(15)\n\t"
      "v_mfma_f32_32x32x16_bf16 v[68:83], a[52:55], v[40:43], v[68:83]\n\t"
      "ds_read_b128 v[40:43], %[b] offset:21504\n\t"
      "s_waitcnt lgkmcnt(15)\n\t"
      "v_mfma_f32_32x32x16_bf16 v[68:83], a[56:59], v[44:47], v[68:83]\n\t"
      "ds_read_b128 v[44:47], %[b] offset:22528\n\t"
      "s_waitcnt lgkmcnt(15)\n\t"
      "v_mfma_f32_32x32x16_bf16 v[68:83], a[60:63], v[48:51], v[68:83]\n\t"
      "ds_read_b128 v[48:51], %[b] offset:23552\n\t"
      "s_waitcnt lgkmcnt(15)\n\t"
      "v_mfma_f32_32x32x16_bf16 v[68:83], a[64:67], v[20:23], v[68:83]\n\t"
      "ds_read_b128 v[20:23], %[b] offset:24576\n\t"
      "s_waitcnt lgkmcnt(13)\n\t"
      "v_mfma_f32_32x32x16_bf16 v[68:83], a[68:71], v[24:27], v[68:83]\n\t"
      "ds_read_b128 v[24:27], %[b] offset:25600\n\t"
      "s_waitcnt lgkmcnt(11)\n\t"
      "v_mfma_f32_32x32x16_bf16 v[68:83], a[72:75], v[28:31], v[68:83]\n\t"
      "ds_read_b128 v[28:31], %[b] offset:26624\n\t"
      "s_waitcnt lgkmcnt(9)\n\t"
      "v_mfma_f32_32x32x16_bf16 v[68:83], a[76:79], v[32:35], v[68:83]\n\t"
      "ds_read_b128 v[32:35], %[b] offset:27648\n\t"
      "s_waitcnt lgkmcnt(7)\n\t"
      "v_mfma_f32_32x32x16_bf16 v[68:83], a[0:3], v[36:39], v[68:83]\n\t"
      "ds_read_b128 v[36:39], %[b] offset:28672\n\t"
      "s_waitcnt lgkmcnt(7)\n\t"
      "v_mfma_f32_32x32x16_bf16 v[68:83], a[4:7], v[40:43], v[68:83]\n\t"
      "ds_read_b128 v[40:43], %[b] offset:29696\n\t"
      "s_waitcnt lgkmcnt(7)\n\t"
      "v_mfma_f32_32x32x16_bf16 v[68:83], a[8:11], v[44:47], v[68:83]\n\t"
      "ds_read_b128 v[44:47], %[b] offset:30720\n\t"
      "s_waitcnt lgkmcnt(7)\n\t"
      "v_mfma_f32_32x32x16_bf16 v[68:83], a[12:15], v[48:51], v[68:83]\n\t"
      "ds_read_b128 v[48:51], %[b] offset:31744\n\t"
      "s_waitcnt lgkmcnt(7)\n\t"
      "v_mfma_f32_32x32x16_bf16 v[68:83], a[16:19], v[20:23], v[68:83]\n\t"
      "s_waitcnt lgkmcnt(6)\n\t"
      "v_mfma_f32_32x32x16_bf16 v[68:83], a[20:23], v[24:27], v[68:83]\n\t"
      "s_waitcnt lgkmcnt(5)\n\t"
      "v_mfma_f32_32x32x16_bf16 v[68:83], a[24:27], v[28:31], v[68:83]\n\t"
      "s_waitcnt lgkmcnt(4)\n\t"
      "v_mfma_f32_32x32x16_bf16 v[68:83], a[28:31], v[32:35], v[68:83]\n\t"
      "s_waitcnt lgkmcnt(3)\n\t"
      "v_mfma_f32_32x32x16_bf16 v[68:83], a[32:35], v[36:39], v[68:83]\n\t"
      "s_waitcnt lgkmcnt(2)\n\t"
      "v_mfma_f32_32x32x16_bf16 v[68:83], a[36:39], v[40:43], v[68:83]\n\t"
      "s_waitcnt lgkmcnt(1)\n\t"
      "v_mfma_f32_32x32x16_bf16 v[68:83], a[40:43], v[44:47], v[68:83]\n\t"
      "s_waitcnt lgkmcnt(0)\n\t"
      "v_mfma_f32_32x32x16_bf16 v[68:83], a[44:47], v[48:51], v[68:83]\n\t"
      "s_nop 15\n\t"
      "s_nop 3\n\t"
      : "=&{v[68:83]}"(a) : [b] "v"(L_OS + vk), [a1] "v"(abase + 65536) : "memory", ACLOB, "v20","v21","v22","v23","v24","v25","v26","v27","v28","v29","v30","v31","v32","v33","v34","v35","v36","v37","v38","v39","v40","v41","v42","v43","v44","v45","v46","v47","v48","v49","v50","v51");
    float r[16];
#pragma unroll
    for (int i = 0; i < 16; ++i) r[i] = fmaf(gate, a[i], tb[i]);
    *(u32x4*)(out + (size_t)token * D_ + col) = u32x4{cvtpk(r[0], r[1]), cvtpk(r[2], r[3]), cvtpk(r[4], r[5]), cvtpk(r[6], r[7])};
    *(u32x4*)(out + (size_t)token * D_ + col + 8) = u32x4{cvtpk(r[8], r[9]), cvtpk(r[10], r[11]), cvtpk(r[12], r[13]), cvtpk(r[14], r[15])};
    if (b == 0 && w == 0 && lane == 0) ST64((u32x2{tlo, thi}), WS_EP);
  }
  ST(11);
}

__global__ void __launch_bounds__(576, 3)
fused(const u16* __restrict__ x, const u16* __restrict__ W, const u16* __restrict__ wo, const u16* __restrict__ wr,
      const u16* __restrict__ wg, const u16* __restrict__ wu, const u16* __restrict__ wd, const u16* __restrict__ gw,
      char* __restrict__ ws, u16* __restrict__ out) {
  u64 kstart; asm volatile("s_getpc_b64 %0" : "=s"(kstart));
  constexpr unsigned l0 = 0;
  const int b = blockIdx.x;
  int hg, rb; MAPB(b, hg, rb);
  const int lane = threadIdx.x & 63, w = __builtin_amdgcn_readfirstlane(threadIdx.x >> 6);
  const int c31 = lane & 31, hh = lane >> 5, blk = (lane >> 4) & 1, q4 = (lane & 15) >> 2, p4 = lane & 3;
  const int tt = 2 * rb + (hg >> 2), cg = hg & 3;
  const __amdgpu_buffer_rsrc_t rs = __builtin_amdgcn_make_buffer_rsrc(ws, 0, (int)WS_END, 0x00020000);
  STDECL
  ST(0);
  if (w == 7 && (b >> 3) < 22) {
    const char* cb = (const char*)(kstart & ~0xffull) + (b >> 3) * 1024 + lane * 16;
    asm volatile("global_load_dwordx4 v[40:43], %0, off" :: "v"(cb) : "memory", "v40","v41","v42","v43");
  }
  asm volatile("" :: "s"(x), "s"(W), "s"(wr), "s"(gw), "s"(ws));
#define GET_TAG() u64 ep_; asm volatile("s_load_dwordx2 %0, %1, 0x0\n\ts_waitcnt lgkmcnt(0)" : "=s"(ep_) : "s"(ws + WS_EP) : "memory"); \
  const u64 tag = ep_ * 0x9E3779B97F4A7C15ull + 0xD1B54A32D192ED03ull; const u32 thi = (u32)(tag >> 32), tlo = (u32)tag

  if (w == 8) {
    {
      const char* p;
      if (lane < 14) p = (const char*)wg + lane * 131072;
      else if (lane < 28) p = (const char*)wu + (lane - 14) * 131072;
      else if (lane < 42) p = (const char*)wd + (lane - 28) * 131072;
      else if (lane < 46) p = (const char*)wo + (lane - 42) * 131072;
      else if (lane < 52) p = ws + WS_KV + hg * 8 * 32 * KVB + (lane - 46) * 111104;
      else if (lane < 53) p = ws + WS_OF + tt * 32768;
      else if (lane < 54) p = ws + WS_FL;
      else if (lane < 55) p = ws + WS_Y;
      else p = (const char*)out + tt * 32768;
      asm volatile("global_load_dword v52, %0, off" :: "v"(p) : "memory", "v52");
    }
#pragma unroll
    for (int i = 0; i < 8; ++i) ST128((u32x4{~0u, ~0u, ~0u, ~0u}), WS_OF + ((2 * rb + (i >> 2)) * 32 + 4 * hg + (i & 3)) * 1024 + lane * 16);
    if (lane < 4) LDSU(L_CTL + lane * 4) = 0;
    GET_TAG();
#pragma unroll 1
    for (int i = 0; i < 6; ++i) BAR();
    DRAIN();
    if (lane == 0) ST64((u32x2{tlo, thi}), WS_OL + (rb * 8 + hg) * 128);
#pragma unroll 1
    for (int i = 0; i < 5; ++i) BAR();
    const int cg1 = (b & 7) * 28 + (b >> 3);
    const int ks2 = b & 7, cg2 = b >> 3;
    const bool has1 = (b >> 3) < 28;
    ST(2);
    __builtin_amdgcn_s_setprio(HPRIO);
    const rsrc_t ro = mkrs(wo, D_ * D_ * 2);
    const unsigned vwo = (lane >> 4) * (D_ * 2) + cg * 256 + (((lane & 15) ^ ((lane >> 4) << 2)) * 16);
    unsigned key = 0;
    for (int spin = 0; spin < SPIN_MAX; ++spin) {
      if (spin) __builtin_amdgcn_s_sleep(2);
      FRESH();
      const u32x2 f = LD64(WS_FL + (hg * 32 + ((lane + rb) & 31)) * 128);
      key = f[0] & 0x7ffffffu;
      const unsigned rdy = (unsigned)__ballot(f[1] == thi && (f[0] ^ tlo) < 0x8000000u);
      if (lane == 0) LDSU(L_CTL) = rdy;
      if (rdy == ~0u) break;
    }
    if (lane == 0) LDSU(L_CTL) = ~0u;
    ST(3);
    const unsigned best = wave_max_u32(key);
    const int sel = 2047 - (int)(best & 2047u);
    if (lane == 0) LDSU(L_CTL + 12) = best | 0x80000000u;
    if (has1) {
      const rsrc_t rg = mkrs(wg, D_ * F_ * 2), ru = mkrs(wu, D_ * F_ * 2);
      const unsigned vo = (lane >> 1) * (F_ * 2) + cg1 * 16;
#pragma unroll 1
      for (int i = 0; i < 16; ++i)
        asm volatile("s_mov_b32 m0, %0\n\ts_mov_b64 exec, %5\n\tbuffer_load_dwordx4 %1, %2, %4 offen lds\n\ts_not_b64 exec, exec\n\t"
                     "buffer_load_dwordx4 %1, %3, %4 offen lds\n\ts_mov_b64 exec, -1"
                     :: "s"(L_GU + i * 1024), "v"(vo), "s"(rg), "s"(ru), "s"(i * 32 * F_ * 2), "s"(0x5555555555555555ull) : "memory", "scc");
    }
    {
      const rsrc_t rd = mkrs(wd, D_ * F_ * 2);
      const unsigned vo = (ks2 * 224 + (lane >> 1)) * (D_ * 2) + cg2 * 32 + (lane & 1) * 16;
#pragma unroll 1
      for (int i = 0; i < 7; ++i) dma<L_WD>(vo, rd, i * 32 * D_ * 2, i * 1024);
    }
    if (has1) {
      dma<L_XS>(lane * 16, mkrs(x, 2048 * D_ * 2), sel * (D_ * 2), 0);
      ST(4);
      DRAIN();
      ST(5);
      f32x4 d = {0};
      const unsigned ba = l0 + L_GU + (8 * (lane >> 4) + q4) * 32 + p4 * 8;
#pragma unroll 4
      for (int u = 0; u < 16; ++u)
        d = MFMA16(lds_b128(l0 + L_XS + (4 * u + (lane >> 4)) * 16), cat8(lds_tr(ba + u * 1024), lds_tr(ba + u * 1024 + 128)), d);
      const float gp = bfround(d[0]), up = bfround(__shfl_down(d[0], 8));
      const unsigned hb = cvtpk(bfround(gp * sigmoidf_(gp)) * up, 0.f) & 0xffffu;
      if (lane < 8) ST64((u32x2{(tlo & 0xffff0000u) | hb, thi}), WS_H + (cg1 * 8 + lane) * 8);
    }
    ST(6);
#pragma unroll 1
    for (int i = 25; i < 80; ++i) dma<0>(vwo, ro, i * 4096, i * 1024);
    ST(7);
    for (int spin = 0; spin < SPIN_MAX; ++spin) {
      if (spin) __builtin_amdgcn_s_sleep(4);
      FRESH();
      const unsigned o = WS_H + ks2 * 1792 + (lane < 56 ? lane : 0) * 32;
      const u32x4 a = LD128(o), c = LD128(o + 16);
      const bool ok = a[1] == thi && a[3] == thi && c[1] == thi && c[3] == thi &&
                      ((a[0] ^ tlo) | (a[2] ^ tlo) | (c[0] ^ tlo) | (c[2] ^ tlo)) < 0x10000u;
      *(LAS u32x2*)(size_t)(l0 + L_HS + lane * 8) = u32x2{(a[0] & 0xffffu) | (a[2] << 16), (c[0] & 0xffffu) | (c[2] << 16)};
      if (__all(ok)) break;
    }
    ST(8);
    {
      f32x4 d = {0};
      const unsigned ba = l0 + L_WD + (8 * (lane >> 4) + q4) * 32 + p4 * 8;
#pragma unroll
      for (int u = 0; u < 7; ++u)
        d = MFMA16(lds_b128(l0 + L_HS + (4 * u + (lane >> 4)) * 16), cat8(lds_tr(ba + u * 1024), lds_tr(ba + u * 1024 + 128)), d);
      const unsigned yb = __float_as_uint(d[0]);
      if (lane < 16) ST128((u32x4{(tlo & 0xffff0000u) | (yb & 0xffffu), thi, (tlo & 0xffff0000u) | (yb >> 16), thi}),
                           WS_Y + (ks2 * 512 + cg2 * 16 + lane) * 16);
      asm volatile("s_waitcnt lgkmcnt(0)" ::: "memory");
    }
    ST(9);
#pragma unroll 1
    for (int i = 80; i < 128 + 25; ++i) { const int r = i & 127; dma<0>(vwo, ro, r * 4096, r * 1024); }
    for (int spin = 0; spin < SPIN_MAX; ++spin) {
      if (spin) __builtin_amdgcn_s_sleep(4);
      FRESH();
      const u32x2 f = LD64(WS_OL + (rb * 8 + (lane & 7)) * 128);
      if (__all(f[0] == tlo && f[1] == thi)) break;
    }
    ST(1);
    WAIT_BAR(0);
    BAR();
    STDUMP();
    return;
  }

  const int rot = (rb + hg) & 7;
  const rsrc_t rx = mkrs(x, 2048 * D_ * 2), rw = mkrs(W, D_ * 1536 * 2);
  const unsigned mw = w * 1024;
  const unsigned vx = (rb * 64 + w * 8 + (lane >> 3)) * 1024 + (((lane & 7) ^ ((w * 4 + (lane >> 4)) & 7)) << 4);
  const unsigned vq = ((lane >> 3) + w * 8) * 3072 + hg * 128 + (((lane & 7) ^ (((lane >> 4) & 1) << 2)) << 4);
  const unsigned vkv = vq + (w < 4 ? 1024 : 2048 - 32 * 3072);
  if (w == 4) dma<L_WV>(((lane + 8 * rot) & 63) * 16, mkrs(wr, 1024), 0, 0);
  if (w == 5) dma<L_WV + 1024>(((lane + 8 * rot) & 63) * 16, mkrs(gw, 1024), 0, 0);
#define KBLK(g) ((unsigned)(((g) + rot) & 7))
#define ISSX(g) dma<L_X + (g) * 8192>(vx, rx, KBLK(g) * 128, mw)
#define ISSUE(r) do { if ((r) < 16) dma<L_W + (((r) + 4) % 10) * 8192>(vkv, rw, KBLK((r) >> 1) * 196608 + ((r) & 1) * 98304, mw); \
                      else dma<L_W + (((r) + 4) % 10) * 8192>(vq, rw, KBLK(r) * 196608, mw); } while (0)
#define ISSG(g) do { ISSX(g); ISSUE(2 * (g)); ISSUE(2 * (g) + 1); } while (0)
#define LEAD 4
  ISSG(0); ISSG(1); ISSG(2);
  if (LEAD == 4) ISSG(3);

  const int part = w >> 2, pair = w & 1, tile = (w >> 1) & 1;
  unsigned xa0 = L_X + (tile * 32 + c31) * 128 + ((hh ^ ((c31 >> 1) & 7)) << 4);
  unsigned xa1 = xa0 ^ 32, xa2 = xa0 ^ 64, xa3 = xa0 ^ 96;
  unsigned wq0 = L_W + (8 * hh + q4) * 128 + (((4 * pair + 2 * blk + (p4 >> 1)) ^ (((q4 >> 1) & 1) << 2)) << 4) + 8 * (p4 & 1);
  unsigned wa0 = wq0 + part * 4096, wa1 = wa0 + 65536;
  asm volatile("" : "+v"(xa0), "+v"(xa1), "+v"(xa2), "+v"(xa3), "+v"(wa0), "+v"(wa1), "+v"(wq0));
  f32x16 acc = {0};
  GET_TAG();
  {
    bf16x8 wf[2][4], xf[2][4];
#define WADDR(g, u) ((((2 * (g) + 4 + ((u) >> 1)) % 10) >= 8 ? wa1 - 65536 : wa0) + ((2 * (g) + 4 + ((u) >> 1)) % 10) * 8192 + ((u) & 1) * 2048)
#define RD1(g, u, xa) do { xf[(g) & 1][u] = lds_b128(xa + (g) * 8192); \
      wf[(g) & 1][u] = cat8(lds_tr(WADDR(g, u)), lds_tr(WADDR(g, u) + 512)); } while (0)
#define RD4(g) do { RD1(g, 0, xa0); RD1(g, 1, xa1); RD1(g, 2, xa2); RD1(g, 3, xa3); } while (0)
#define MM(V, g, u) do { if (V) acc = MFMA32(xf[(g) & 1][u], wf[(g) & 1][u], acc); else acc = MFMA32(wf[(g) & 1][u], xf[(g) & 1][u], acc); SBAR(); } while (0)
#define NXT(g, i) do { if ((g) + LEAD <= 7) { if ((i) == 0) ISSX(((g) + LEAD) & 7); else ISSUE(2 * ((g) + LEAD) + (i) - 1); } \
      if ((g) >= 4 && (i) > 0) ISSUE(2 * (g) + 7 + (i)); } while (0)
#define GROUP(V, g, N) do { WAIT_BAR(N); STG(4 + (g)); MM(V, (g) - 1, 0); RD1(g, 0, xa0); NXT(g, 0); SBAR(); MM(V, (g) - 1, 1); RD1(g, 1, xa1); NXT(g, 1); SBAR(); \
      MM(V, (g) - 1, 2); RD1(g, 2, xa2); NXT(g, 2); SBAR(); MM(V, (g) - 1, 3); RD1(g, 3, xa3); SBAR(); } while (0)
#define WB0 9
#define KVLOOP_(V, N) do { WAIT_BAR(N); ST(1); RD4(0); NXT(0, 0); NXT(0, 1); NXT(0, 2); SBAR(); GROUP(V, 1, N); GROUP(V, 2, N); GROUP(V, 3, N); GROUP(V, 4, N); \
      GROUP(V, 5, 8); GROUP(V, 6, 7); if (LEAD == 4) GROUP(V, 7, 6); else GROUP(V, 7, 5); MM(V, 7, 0); MM(V, 7, 1); MM(V, 7, 2); MM(V, 7, 3); } while (0)
#define KVLOOP__(V, N) KVLOOP_(V, N)
#define KVLOOP(V) KVLOOP__(V, WB0)
    if (part == 0) KVLOOP(false); else KVLOOP(true);
  }
  ST(2);
  const u32x4 p0 = pack8(acc, 0), p1 = pack8(acc, 1);
  unsigned o0, o1;
  int h0, h1;
  if (part == 0) {
    const int s23 = (c31 & 19) | ((c31 & 4) << 1) | ((c31 & 8) >> 1);
    o0 = o1 = (s23 + 32 * hh) * 16; h0 = 2 * pair; h1 = 2 * pair + 1;
  } else {
    o0 = 1024 + (hh * 16 + (lane & 15)) * 16; o1 = o0 + 512; h0 = h1 = 2 * pair + blk;
  }
  ST128(p0, WS_KV + (((4 * hg + h0) * 2 + tile) * 32 + rb) * KVB + o0);
  ST128(p1, WS_KV + (((4 * hg + h1) * 2 + tile) * 32 + rb) * KVB + o1);
  if (part == 1 && lane < 2) ST128((u32x4{0x3F803F80u, 0x3F803F80u, 0x3F803F80u, 0x3F803F80u}), WS_KV + (((4 * hg + 2 * pair + lane) * 2 + tile) * 32 + rb) * KVB + 2048);
  if (part == 0) WAIT_BAR(2); else WAIT_BAR(3);
  ST(13);
  lds_w128(l0 + L_KV + (h0 * 2 + tile) * 2048 + o0, p0);
  lds_w128(l0 + L_KV + (h1 * 2 + tile) * 2048 + o1, p1);
  if (part == 0) {
    f32x16 qacc = {0};
    bf16x8 wf[2][4], xf[2][4];
#define QR1(r, u, xa) do { xf[(r) & 1][u] = lds_b128(xa + (r) * 8192); \
      wf[(r) & 1][u] = cat8(lds_tr(wq0 + (r) * 8192 + (u) * 2048), lds_tr(wq0 + (r) * 8192 + (u) * 2048 + 512)); } while (0)
#define QR4(r) do { QR1(r, 0, xa0); QR1(r, 1, xa1); QR1(r, 2, xa2); QR1(r, 3, xa3); } while (0)
    QR4(0);
    SBAR();
#define QS(r, u, xa) do { qacc = MFMA32(wf[(r) & 1][u], xf[(r) & 1][u], qacc); SBAR(); if ((r) < 7) QR1((r) + 1, u, xa); SBAR(); } while (0)
#define QG(r) do { QS(r, 0, xa0); QS(r, 1, xa1); QS(r, 2, xa2); QS(r, 3, xa3); } while (0)
    QG(0); QG(1); QG(2); QG(3); QG(4); QG(5); QG(6); QG(7);
    const float qs = 0.25f * 1.4426950408889634f;
    lds_w128(l0 + L_QX + ((2 * pair) * 2 + tile) * 1024 + lane * 16, pack8(qacc, 0, qs));
    lds_w128(l0 + L_QX + ((2 * pair + 1) * 2 + tile) * 1024 + lane * 16, pack8(qacc, 1, qs));
  } else {
    const int tok = 16 * (w & 3) + (lane & 15), g4 = lane >> 4;
    f32x4 d = {0};
    unsigned fa0 = L_WV + (lane & 1) * 1024 + g4 * 16, fb0 = L_X + tok * 128 + ((g4 ^ ((tok >> 1) & 7)) << 4), fb1 = fb0 ^ 64;
    asm volatile("" : "+v"(fa0), "+v"(fb0), "+v"(fb1));
    bf16x8 fa[16], fb[16];
#define RGR(u) do { fa[u] = lds_b128(fa0 + (u) * 64); fb[u] = lds_b128((((u) & 1) ? fb1 : fb0) + ((u) >> 1) * 8192); } while (0)
    RGR(0); RGR(1); RGR(2); RGR(3);
    SBAR();
#pragma unroll
    for (int u = 0; u < 16; ++u) { d = MFMA16(fa[u], fb[u], d); SBAR(); if (u + 4 < 16) RGR((u + 4) & 15); SBAR(); }
    const unsigned r16 = cvtpk(d[0], 0.f) & 0xffffu;
    unsigned key = (((r16 & 0x8000u) ? (~r16 & 0xffffu) : (r16 | 0x8000u)) << 11) | (2047 - (rb * 64 + tok));
    if (lane < 16) {
      asm volatile("ds_max_u32 %0, %1" :: "v"(L_CTL + 8), "v"(key) : "memory");
      *(LAS float*)(size_t)(l0 + L_RG + tok * 4) = bfround(sigmoidf_(bfround(d[1])));
    }
    if (w == 7 && lane < 2) { const unsigned v = lane ? 0u : 0x3F803F80u; lds_w128(l0 + L_ONE + lane * 16, u32x4{v, v, v, v}); }
  }
  ST(3);
  BAR();
  DRAIN();
  if (lane == 0 && __hip_atomic_fetch_add((LAS u32*)(size_t)(L_CTL + 4), 1u, __ATOMIC_RELAXED, __HIP_MEMORY_SCOPE_WORKGROUP) == 7)
    ST64((u32x2{(tlo & 0xf8000000u) | LDSU(L_CTL + 8), thi}), WS_FL + (hg * 32 + rb) * 128);
  ST(14);

  const int j = w & 3, kh = w >> 2, head = 4 * hg + j;
  const bool onerow = c31 == 16 || c31 == 20;
  const unsigned idx = 0xDDDDDDDDu, vk = lane * 16;
  const unsigned vv0 = c31 < 16 ? 1024 + (hh * 16 + c31) * 16 : (onerow ? 2048u : 0x7f000000u);
  const unsigned vv1 = c31 < 16 ? vv0 + 512 : vv0;
  const bf16x8 qa = lds_b128(l0 + L_QX + (j * 2) * 1024 + lane * 16), qb = lds_b128(l0 + L_QX + (j * 2 + 1) * 1024 + lane * 16);
  const float gate = *(const LAS float*)(size_t)(l0 + L_RG + ((hg >> 2) * 32 + c31) * 4);
  f32x16 sa, sb, mm;
  {
    const unsigned lk = l0 + L_KV + (j * 2 + kh) * 2048;
    const unsigned la = lk + vk, lm = l0 + L_KV + j * 4096 + vk;
    const unsigned lv0 = c31 < 16 ? lk + vv0 : l0 + L_ONE + (onerow ? 0 : 16), lv1 = c31 < 16 ? lv0 + 512 : lv0;
#define Z1(r) "v_accvgpr_write_b32 a[" STR(r) "], 0\n\t"
#define Z4(r) Z1(r) Z1(r+1) Z1(r+2) Z1(r+3)
#define Z16(r) Z4(r) Z4(r+4) Z4(r+8) Z4(r+12)
    asm volatile("ds_read_b128 " KS(1) ", %[lm]\n\tds_read_b128 " KS(0) ", %[la]\n\tds_read_b128 " V0(0) ", %[a0]\n\tds_read_b128 " V1(0) ", %[a1]\n\t"
                 Z16(0) Z16(16)
                 "s_waitcnt lgkmcnt(0)\n\ts_barrier\n\t"
                 "v_mfma_f32_32x32x16_bf16 v[36:51], " KS(1) ", %[qa], 0\n\t"
                 "v_mfma_f32_32x32x16_bf16 v[52:67], " KS(1) ", %[qb], 0\n\t"
                 "s_nop 15\n\ts_nop 3\n\t"
                 : "=&{v[36:51]}"(sa), "=&{v[52:67]}"(sb)
                 : [lm] "v"(lm), [la] "v"(la), [a0] "v"(lv0), [a1] "v"(lv1), [qa] "v"(qa), [qb] "v"(qb)
                 : "memory", ACLOB);
    const float nm = -halves_max(fmaxf(max16(sa), max16(sb)));
#pragma unroll
    for (int i = 0; i < 16; ++i) mm[i] = nm;
    asm volatile("s_nop 1\n\t" QKA(0) QKB(0) "s_nop 11\n\t"
                 EX4(36) EX4(40) EX4(44) SMF(OA, V0(0), 36) EX4(48) EX4(52) SMF(OA, V1(0), 44) EX4(56) EX4(60) SMF(OB, V0(0), 52) EX4(64)
                 : "=&{v[36:51]}"(sa), "=&{v[52:67]}"(sb)
                 : "{v[68:83]}"(mm), [qa] "v"(qa), [qb] "v"(qb), [idx] "v"(idx) : "memory", ACLOB);
  }
  ST(4);
  ST(12);
  const unsigned kvb = WS_KV + (head * 2 + kh) * 32 * KVB;
  const unsigned tbl = kvb + ((lane + rb) & 31) * KVB;
  unsigned todo = 0xfffffffeu, ready = 0;
  int spins = 0;
  auto pick = [&]() -> unsigned {
    while (!(ready & todo)) {
      if (spins) __builtin_amdgcn_s_sleep(1);
      ready = __builtin_amdgcn_readfirstlane(LDSU(L_CTL));
      if (++spins > (SPIN_MAX << 4)) ready = ~0u;
    }
    const int i = __builtin_ctz(ready & todo);
    todo &= ~(1u << i);
    return __builtin_amdgcn_readlane(tbl, i);
  };
  unsigned s3, s4, s5, s6, s7;
  {
    const unsigned s1 = pick(), s2 = pick();
    s3 = pick(); s4 = pick();
    ST(5);
    asm volatile("s_nop 4\n\t" LDK(1, "%[s1]") LDV(1, "%[s1]") LDK(2, "%[s2]") LDV(2, "%[s2]") LDK(3, "%[s3]") LDV(3, "%[s3]") LDK(0, "%[s4]")
                 "s_waitcnt vmcnt(9)\n\t" QKA(1) "s_nop 15\n\t"
                 : "=&{v[36:51]}"(sa)
                 : "{v[68:83]}"(mm), [qa] "v"(qa), [vk] "v"(vk), [v0] "v"(vv0), [v1] "v"(vv1), [rs] "s"(rs),
                   [s1] "s"(s1), [s2] "s"(s2), [s3] "s"(s3), [s4] "s"(s4)
                 : "memory", ACLOB);
  }
  ST(6);
  __builtin_amdgcn_s_setprio(2);
  s5 = pick(); TILE(1, 0, 2, s4, s5);
  s6 = pick(); TILE(2, 1, 3, s5, s6);
  s7 = pick(); TILE(3, 2, 0, s6, s7);
#pragma unroll 1
  for (int t = 4; t < 28; t += 4) {
    if (t == 20) __builtin_amdgcn_s_setprio(1);
    s4 = pick(); TILE(0, 3, 1, s7, s4);
    s5 = pick(); TILE(1, 0, 2, s4, s5);
    s6 = pick(); TILE(2, 1, 3, s5, s6);
    s7 = pick(); TILE(3, 2, 0, s6, s7);
  }
  TILE(0, 3, 1, s7, s7);
  TILE(1, 0, 2, s7, s7);
  __builtin_amdgcn_s_setprio(0);
  TILE(2, 1, 3, s7, s7);
  TILE(3, 2, 0, s7, s7);
  float oa[9], ob[9];
#define RD(i, r) "v_accvgpr_read_b32 %" STR(i) ", a" STR(r) "\n\t"
  asm volatile("s_nop 1\n\t" SMF(OB, V1(3), 60) "s_waitcnt vmcnt(0)\n\ts_nop 15\n\ts_nop 3\n\t"
               RD(0, 0) RD(1, 1) RD(2, 2) RD(3, 3) RD(4, 4) RD(5, 5) RD(6, 6) RD(7, 7) RD(8, 8)
               RD(9, 16) RD(10, 17) RD(11, 18) RD(12, 19) RD(13, 20) RD(14, 21) RD(15, 22) RD(16, 23) RD(17, 24)
               : "=v"(oa[0]), "=v"(oa[1]), "=v"(oa[2]), "=v"(oa[3]), "=v"(oa[4]), "=v"(oa[5]), "=v"(oa[6]), "=v"(oa[7]), "=v"(oa[8]),
                 "=v"(ob[0]), "=v"(ob[1]), "=v"(ob[2]), "=v"(ob[3]), "=v"(ob[4]), "=v"(ob[5]), "=v"(ob[6]), "=v"(ob[7]), "=v"(ob[8])
               : "{v[52:67]}"(sb), [idx] "v"(idx) : "memory", ACLOB);
  ST(7);
  TAIL(ws, x, out, rs, b, w, oa, ob, qa, qb, vk, vv0, vv1, idx, gate, tlo, thi STPASS);
  STDUMP();
}

extern "C" void kernel_launch(void* const* d_in, const int* in_sizes, int n_in, void* d_out, int out_size, void* d_ws, size_t ws_size,
                              hipStream_t stream) {
  (void)hipFuncSetAttribute((const void*)fused, hipFuncAttributeMaxDynamicSharedMemorySize, LDS_BYTES);
  fused<<<256, 576, LDS_BYTES, stream>>>((const u16*)d_in[0], (const u16*)d_in[1], (const u16*)d_in[2], (const u16*)d_in[4],
                                         (const u16*)d_in[5], (const u16*)d_in[6], (const u16*)d_in[7], (const u16*)d_in[8],
                                         (char*)d_ws, (u16*)d_out);
}
```

```cpp
#define DIAG 99
#define DIAGW 1
#include <hip/hip_runtime.h>
#include <hip/hip_bf16.h>

typedef unsigned short u16;
typedef unsigned int u32;
typedef unsigned long long u64;
typedef __attribute__((ext_vector_type(8))) __bf16 bf16x8;
typedef __attribute__((ext_vector_type(16))) __bf16 bf16x16;
typedef __attribute__((ext_vector_type(16))) float f32x16;
typedef __attribute__((ext_vector_type(8))) float f32x8;
typedef __attribute__((ext_vector_type(4))) float f32x4;
typedef __attribute__((ext_vector_type(4))) unsigned u32x4;
typedef __attribute__((ext_vector_type(2))) unsigned u32x2;
typedef __attribute__((ext_vector_type(4))) short s16x4;
#define LAS __attribute__((address_space(3)))

constexpr int D_ = 512, F_ = 1792;
constexpr u32 KVB = 2176;
constexpr u32 WS_KV = 0;
constexpr u32 WS_OF = 8u << 20;
constexpr u32 WS_FL = 10u << 20;
constexpr u32 WS_OL = WS_FL + 32768;
constexpr u32 WS_H  = WS_OL + 32768;
constexpr u32 WS_Y  = WS_H + 16384;
constexpr u32 WS_EP = WS_Y + 65536;
constexpr u32 WS_END = WS_EP + 128;
constexpr u32 L_X = 0, L_W = 65536, L_KV = 131072, L_WV = 147456, L_QX = 149504, L_RG = 157696, L_ONE = 158208;
constexpr u32 L_GU = 0, L_WD = 16384, L_XS = 23552, L_HS = 24576;
constexpr u32 L_OS = 131072;
constexpr u32 L_CTL = 163808;
constexpr int LDS_BYTES = 163840;
constexpr int SPIN_MAX = 1 << 18;

#define STG(i)
struct Stamps { u64 t0, t; };
#define ST(i) do { if ((i) == DIAG) { asm volatile("s_memrealtime %0\n\ts_waitcnt lgkmcnt(0)" : "=s"(stp.t) :: "memory"); } } while (0)
#define STDECL
#define STDUMP() do { if (blockIdx.x == 77 && (threadIdx.x >> 6) == DIAGW) { const u64 end = stp.t0 + 3500 + 2 * (stp.t - stp.t0); while (__builtin_amdgcn_s_memrealtime() < end) __builtin_amdgcn_s_sleep(1); } } while (0)
#define STARG , Stamps& stp
#define STPASS , stp

#define HPRIO 3
#define MAP 3
#define MAPB(b, hg, rb) do { if (MAP == 3) { hg = 2 * (b & 3) + ((b >> 3) & 1); rb = 16 * ((b >> 2) & 1) + (b >> 4); } \
  else if (MAP == 2) { hg = 4 * (b & 1) + ((b >> 3) & 3); rb = 8 * ((b >> 1) & 3) + (b >> 5); } \
  else if (MAP == 1) { hg = b & 7; rb = b >> 3; } else { hg = (b >> 3) & 7; rb = 4 * (b & 7) + (b >> 6); } } while (0)
typedef __amdgpu_buffer_rsrc_t rsrc_t;
__device__ __forceinline__ rsrc_t mkrs(const void* p, unsigned bytes) { return __builtin_amdgcn_make_buffer_rsrc((void*)p, 0, (int)bytes, 0x00020000); }
template <unsigned ADD>
__device__ __forceinline__ void dma(unsigned voff, rsrc_t r, unsigned soff, unsigned m) {
  asm volatile("s_add_u32 m0, %0, %c4\n\ts_nop 0\n\tbuffer_load_dwordx4 %1, %2, %3 offen lds" :: "s"(m), "v"(voff), "s"(r), "s"(soff), "i"(ADD) : "memory", "scc");
}
__device__ __forceinline__ unsigned cvtpk(float lo, float hi) {
  typedef __attribute__((ext_vector_type(2))) __bf16 bf16x2;
  typedef __attribute__((ext_vector_type(2))) float f32x2;
  f32x2 v = {lo, hi};
  return __builtin_bit_cast(unsigned, __builtin_convertvector(v, bf16x2));
}
__device__ __forceinline__ float bfround(float f) { return __uint_as_float(cvtpk(f, 0.f) << 16); }
__device__ __forceinline__ float bflo(unsigned u) { return __uint_as_float(u << 16); }
__device__ __forceinline__ float bfhi(unsigned u) { return __uint_as_float(u & 0xffff0000u); }
__device__ __forceinline__ float sigmoidf_(float z) { return __builtin_amdgcn_rcpf(1.f + __expf(-z)); }
__device__ __forceinline__ bf16x8 lds_b128(unsigned a) { return *(const LAS bf16x8*)(size_t)a; }
__device__ __forceinline__ void lds_w128(unsigned a, u32x4 v) { *(LAS u32x4*)(size_t)a = v; }
__device__ __forceinline__ s16x4 lds_tr(unsigned a) { return __builtin_amdgcn_ds_read_tr16_b64_v4i16((LAS s16x4*)(size_t)a); }
__device__ __forceinline__ bf16x8 cat8(s16x4 a, s16x4 b) {
  typedef __attribute__((ext_vector_type(8))) short s16x8;
  s16x8 r = {a[0], a[1], a[2], a[3], b[0], b[1], b[2], b[3]};
  return __builtin_bit_cast(bf16x8, r);
}
__device__ __forceinline__ u32x4 pack8(const f32x16& a, int z, float s) {
  return u32x4{cvtpk(a[8 * z] * s, a[8 * z + 1] * s), cvtpk(a[8 * z + 2] * s, a[8 * z + 3] * s),
               cvtpk(a[8 * z + 4] * s, a[8 * z + 5] * s), cvtpk(a[8 * z + 6] * s, a[8 * z + 7] * s)};
}
__device__ __forceinline__ u32x4 pack8(const f32x16& a, int z) {
  return u32x4{cvtpk(a[8 * z], a[8 * z + 1]), cvtpk(a[8 * z + 2], a[8 * z + 3]), cvtpk(a[8 * z + 4], a[8 * z + 5]), cvtpk(a[8 * z + 6], a[8 * z + 7])};
}
__device__ __forceinline__ unsigned wave_max_u32(unsigned v) {
#pragma unroll
  for (int m = 32; m >= 1; m >>= 1) v = max(v, (unsigned)__shfl_xor((int)v, m));
  return __builtin_amdgcn_readfirstlane(v);
}
#define MFMA32(a, b, c) __builtin_amdgcn_mfma_f32_32x32x16_bf16(a, b, c, 0, 0, 0)
#define MFMA16(a, b, c) __builtin_amdgcn_mfma_f32_16x16x32_bf16(a, b, c, 0, 0, 0)
#define WAIT_BAR(N) asm volatile("s_waitcnt vmcnt(" #N ") lgkmcnt(0)\n\ts_barrier" ::: "memory")
#define BAR() asm volatile("s_waitcnt lgkmcnt(0)\n\ts_barrier" ::: "memory")
#define DRAIN() asm volatile("s_waitcnt vmcnt(0)" ::: "memory")
#define SBAR() __builtin_amdgcn_sched_barrier(0)
#define FRESH() asm volatile("" ::: "memory")
#define LDSU(a) (*(volatile LAS u32*)(size_t)(a))
#define LD128(off) __builtin_amdgcn_raw_buffer_load_b128(rs, (off), 0, 16)
#define LD64(off) __builtin_amdgcn_raw_buffer_load_b64(rs, (off), 0, 16)
#define ST128(v, off) __builtin_amdgcn_raw_buffer_store_b128((v), rs, (off), 0, 16)
#define ST64(v, off) __builtin_amdgcn_raw_buffer_store_b64((v), rs, (off), 0, 16)

#define STR_(x) #x
#define STR(x) STR_(x)
#define EX1(b) "v_exp_f32 v[" STR(b) "], v[" STR(b) "]\n\t"
#define EX4(b) EX1(b) EX1(b+1) EX1(b+2) EX1(b+3)
#define AR4(b) "a[" STR(b) ":" STR(b) "+3]"
#define KS(s) AR4(32+4*s)
#define V0(s) AR4(48+8*s)
#define V1(s) AR4(52+8*s)
#define SMF(acc, va, sb) "v_smfmac_f32_32x32x32_bf16 " acc ", " va ", v[" STR(sb) ":" STR(sb) "+7], %[idx]\n\t"
#define OA "a[0:15]"
#define OB "a[16:31]"
#define QKA(s) "v_mfma_f32_32x32x16_bf16 v[36:51], " KS(s) ", %[qa], v[68:83]\n\t"
#define QKB(s) "v_mfma_f32_32x32x16_bf16 v[52:67], " KS(s) ", %[qb], v[68:83]\n\t"
#define LDK(s, so) "buffer_load_dwordx4 " KS(s) ", %[vk], %[rs], " so " offen sc1\n\t"
#define LDV(s, so) "buffer_load_dwordx4 " V0(s) ", %[v0], %[rs], " so " offen sc1\n\t" \
                   "buffer_load_dwordx4 " V1(s) ", %[v1], %[rs], " so " offen sc1\n\t"
#define ACLOB "a0","a1","a2","a3","a4","a5","a6","a7","a8","a9","a10","a11","a12","a13","a14","a15","a16","a17","a18","a19", \
  "a20","a21","a22","a23","a24","a25","a26","a27","a28","a29","a30","a31","a32","a33","a34","a35","a36","a37","a38","a39", \
  "a40","a41","a42","a43","a44","a45","a46","a47","a48","a49","a50","a51","a52","a53","a54","a55","a56","a57","a58","a59", \
  "a60","a61","a62","a63","a64","a65","a66","a67","a68","a69","a70","a71","a72","a73","a74","a75","a76","a77","a78","a79"
#define TILE(s, sp, sn, soV, soK) \
  asm volatile(EX4(36) SMF(OB, V1(sp), 60) LDV(sp, "%[sv]") EX4(40) QKB(s) LDK(s, "%[sk]") EX4(44) \
               "s_waitcnt vmcnt(9)\n\t" SMF(OA, V0(s), 36) EX4(48) \
               EX4(52) SMF(OA, V1(s), 44) EX4(56) QKA(sn) EX4(60) SMF(OB, V0(s), 52) EX4(64) \
               : "+{v[36:51]}"(sa), "+{v[52:67]}"(sb) \
               : "{v[68:83]}"(mm), [qa] "v"(qa), [qb] "v"(qb), [idx] "v"(idx), [vk] "v"(vk), [v0] "v"(vv0), [v1] "v"(vv1), [rs] "s"(rs), \
                 [sv] "s"(soV), [sk] "s"(soK) : "memory", ACLOB)

__device__ __forceinline__ float max16(const f32x16& p) {
  float a = fmaxf(fmaxf(p[0], p[1]), p[2]), b = fmaxf(fmaxf(p[3], p[4]), p[5]);
  a = fmaxf(fmaxf(a, p[6]), p[7]); b = fmaxf(fmaxf(b, p[8]), p[9]);
  a = fmaxf(fmaxf(a, p[10]), p[11]); b = fmaxf(fmaxf(b, p[12]), p[13]);
  a = fmaxf(fmaxf(a, p[14]), p[15]);
  return fmaxf(a, b);
}
__device__ __forceinline__ float halves_max(float m) {
  auto rr = __builtin_amdgcn_permlane32_swap(__float_as_uint(m), __float_as_uint(m), false, false);
  return fmaxf(__uint_as_float(rr[0]), __uint_as_float(rr[1]));
}

__device__ __forceinline__ void TAIL(char* ws, const u16* x, u16* out, __amdgpu_buffer_rsrc_t rs, int b, int w,
    const float (&oa)[9], const float (&ob)[9], bf16x8 qa, bf16x8 qb, unsigned vk, unsigned vv0, unsigned vv1, unsigned idx,
    float gate, u32 tlo, u32 thi STARG) {
  constexpr unsigned l0 = 0;
  asm volatile("" : "+v"(vk));
  const int lane = vk >> 4;
  int hg, rb; MAPB(b, hg, rb);
  const int c31 = lane & 31, hh = lane >> 5, blk = (lane >> 4) & 1, q4 = (lane & 15) >> 2, p4 = lane & 3;
  const int tt = 2 * rb + (hg >> 2), cg = hg & 3, j = w & 3, kh = w >> 2, head = 4 * hg + j;
#pragma unroll
  for (int i = 0; i < 9; ++i) *(LAS float*)(size_t)(l0 + L_OS + (w ^ 4) * 4096 + i * 256 + lane * 4) = kh ? oa[i] : ob[i];
  unsigned best = 0;
  for (int spin = 0; spin < (SPIN_MAX << 4) && !best; ++spin) best = __builtin_amdgcn_readfirstlane(LDSU(L_CTL + 12));
  best &= 0x7fffffffu;
  BAR();
  float o[9];
  bool bad = false;
#pragma unroll
  for (int i = 0; i < 9; ++i) {
    o[i] = (kh ? ob[i] : oa[i]) + *(const LAS float*)(size_t)(l0 + L_OS + w * 4096 + i * 256 + lane * 4);
    bad |= !(fabsf(o[i]) < 1e30f);
  }
  bad |= !(o[8] > 1e-18f && o[8] < 1e18f);
  if (__any(bad)) {
    const bf16x8 q = kh ? qb : qa;
    const unsigned kb0 = WS_KV + head * 64 * KVB;
    float m = -3.0e38f;
#pragma unroll 1
    for (int t = 0; t < 64; ++t) {
      const f32x16 s = MFMA32(__builtin_bit_cast(bf16x8, LD128(kb0 + t * KVB + vk)), q, (f32x16{0}));
      m = fmaxf(m, max16(s));
    }
    m = -halves_max(m);
    f32x16 c, oc = {0};
#pragma unroll
    for (int i = 0; i < 16; ++i) c[i] = m;
#pragma unroll 1
    for (int t = 0; t < 64; ++t) {
      const f32x16 s = MFMA32(__builtin_bit_cast(bf16x8, LD128(kb0 + t * KVB + vk)), q, c);
      f32x8 e0, e1;
#pragma unroll
      for (int i = 0; i < 8; ++i) { e0[i] = __builtin_amdgcn_exp2f(s[i]); e1[i] = __builtin_amdgcn_exp2f(s[8 + i]); }
      const u32x4 va = __builtin_amdgcn_raw_buffer_load_b128(rs, vv0, kb0 + t * KVB, 16);
      const u32x4 vb = __builtin_amdgcn_raw_buffer_load_b128(rs, vv1, kb0 + t * KVB, 16);
      oc = __builtin_amdgcn_smfmac_f32_32x32x32_bf16(__builtin_bit_cast(bf16x8, va), __builtin_bit_cast(bf16x16, e0), oc, (int)idx, 0, 0);
      oc = __builtin_amdgcn_smfmac_f32_32x32x32_bf16(__builtin_bit_cast(bf16x8, vb), __builtin_bit_cast(bf16x16, e1), oc, (int)idx, 0, 0);
    }
#pragma unroll
    for (int i = 0; i < 9; ++i) o[i] = oc[i];
  }
  {
    const float rl = 1.f / o[8];
    const unsigned lim = 0xfffffffeu;
    ST128((u32x4{min(cvtpk(o[0] * rl, o[1] * rl), lim), min(cvtpk(o[2] * rl, o[3] * rl), lim), min(cvtpk(o[4] * rl, o[5] * rl), lim),
                 min(cvtpk(o[6] * rl, o[7] * rl), lim)}), WS_OF + ((2 * rb + kh) * 32 + head) * 1024 + vk);
  }
  ST(8);

  const int token = tt * 32 + c31, col = cg * 128 + (w & 3) * 32 + hh * 16;
  const int sel = 2047 - (int)(best & 2047u);
  u32x4 xa, xc;
  float yv = 0.f;
  const unsigned abase = (4 * hh + q4) * 256 + (((4 * (w & 3) + 2 * (p4 & 1) + blk) ^ (q4 << 2)) << 4) + 8 * (p4 >> 1);
  if (w < 4) {
    asm volatile(
      "ds_read_b64_tr_b16 a[0:1], %0 offset:0\n\t"
      "ds_read_b64_tr_b16 a[2:3], %0 offset:2048\n\t"
      "ds_read_b64_tr_b16 a[4:5], %0 offset:4096\n\t"
      "ds_read_b64_tr_b16 a[6:7], %0 offset:6144\n\t"
      "ds_read_b64_tr_b16 a[8:9], %0 offset:8192\n\t"
      "ds_read_b64_tr_b16 a[10:11], %0 offset:10240\n\t"
      "ds_read_b64_tr_b16 a[12:13], %0 offset:12288\n\t"
      "ds_read_b64_tr_b16 a[14:15], %0 offset:14336\n\t"
      "ds_read_b64_tr_b16 a[16:17], %0 offset:16384\n\t"
      "ds_read_b64_tr_b16 a[18:19], %0 offset:18432\n\t"
      "ds_read_b64_tr_b16 a[20:21], %0 offset:20480\n\t"
      "ds_read_b64_tr_b16 a[22:23], %0 offset:22528\n\t"
      "ds_read_b64_tr_b16 a[24:25], %0 offset:24576\n\t"
      "ds_read_b64_tr_b16 a[26:27], %0 offset:26624\n\t"
      "ds_read_b64_tr_b16 a[28:29], %0 offset:28672\n\t"
      "ds_read_b64_tr_b16 a[30:31], %0 offset:30720\n\t"
      "ds_read_b64_tr_b16 a[32:33], %0 offset:32768\n\t"
      "ds_read_b64_tr_b16 a[34:35], %0 offset:34816\n\t"
      "ds_read_b64_tr_b16 a[36:37], %0 offset:36864\n\t"
      "ds_read_b64_tr_b16 a[38:39], %0 offset:38912\n\t"
      "ds_read_b64_tr_b16 a[40:41], %0 offset:40960\n\t"
      "ds_read_b64_tr_b16 a[42:43], %0 offset:43008\n\t"
      "ds_read_b64_tr_b16 a[44:45], %0 offset:45056\n\t"
      "ds_read_b64_tr_b16 a[46:47], %0 offset:47104\n\t"
      "ds_read_b64_tr_b16 a[48:49], %0 offset:49152\n\t"
      "ds_read_b64_tr_b16 a[50:51], %0 offset:51200\n\t"
      "ds_read_b64_tr_b16 a[52:53], %0 offset:53248\n\t"
      "ds_read_b64_tr_b16 a[54:55], %0 offset:55296\n\t"
      "ds_read_b64_tr_b16 a[56:57], %0 offset:57344\n\t"
      "ds_read_b64_tr_b16 a[58:59], %0 offset:59392\n\t"
      "ds_read_b64_tr_b16 a[60:61], %0 offset:61440\n\t"
      "ds_read_b64_tr_b16 a[62:63], %0 offset:63488\n\t"
      "ds_read_b64_tr_b16 a[64:65], %1 offset:0\n\t"
      "ds_read_b64_tr_b16 a[66:67], %1 offset:2048\n\t"
      "ds_read_b64_tr_b16 a[68:69], %1 offset:4096\n\t"
      "ds_read_b64_tr_b16 a[70:71], %1 offset:6144\n\t"
      "ds_read_b64_tr_b16 a[72:73], %1 offset:8192\n\t"
      "ds_read_b64_tr_b16 a[74:75], %1 offset:10240\n\t"
      "ds_read_b64_tr_b16 a[76:77], %1 offset:12288\n\t"
      "ds_read_b64_tr_b16 a[78:79], %1 offset:14336\n\t"
      :: "v"(abase), "v"(abase + 65536) : "memory", ACLOB);
    xa = *(const u32x4*)(x + (size_t)token * D_ + col); xc = *(const u32x4*)(x + (size_t)token * D_ + col + 8);
    if ((sel >> 5) == tt) {
      for (int spin = 0; spin < SPIN_MAX; ++spin) {
      if (spin) __builtin_amdgcn_s_sleep(4);
      FRESH();
        bool ok = true; yv = 0.f;
#pragma unroll
        for (int i = 0; i < 4; ++i) {
          const u32x4 a = LD128(WS_Y + ((4 * hh + i) * 512 + cg * 128 + w * 32 + c31) * 16);
          ok &= a[1] == thi && a[3] == thi && ((a[0] ^ tlo) | (a[2] ^ tlo)) < 0x10000u;
          yv += __uint_as_float((a[0] & 0xffffu) | (a[2] << 16));
        }
        if (__all(ok)) break;
      }
      yv += __shfl_xor(yv, 32);
    }
  }
  float tb[16];
  if (w < 4) {
#pragma unroll
    for (int i = 0; i < 4; ++i) { tb[2 * i] = bflo(xa[i]); tb[2 * i + 1] = bfhi(xa[i]); tb[8 + 2 * i] = bflo(xc[i]); tb[9 + 2 * i] = bfhi(xc[i]); }
    if ((sel >> 5) == tt) {
      const unsigned srt = best >> 11;
      const float rsel = bfround(sigmoidf_(bflo((srt & 0x8000u) ? (srt & 0x7fffu) : (~srt & 0xffffu))));
      const float yy = bfround(bfround(yv) * rsel);
#pragma unroll
      for (int i = 0; i < 16; ++i) { const float t = __shfl(yy, hh * 16 + i); if (token == sel) tb[i] = bfround(tb[i] + t); }
    }
    const float og = bfround(1.f - gate);
#pragma unroll
    for (int i = 0; i < 16; ++i) tb[i] *= og;
  }
  {
    u32x4 v[4];
    for (int spin = 0; spin < SPIN_MAX; ++spin) {
      if (spin) __builtin_amdgcn_s_sleep(4);
      FRESH();
      unsigned m = 0;
#pragma unroll
      for (int i = 0; i < 4; ++i) v[i] = LD128(WS_OF + (tt * 32 + 4 * w + i) * 1024 + vk);
#pragma unroll
      for (int i = 0; i < 4; ++i) m = max(max(m, max(v[i][0], v[i][1])), max(v[i][2], v[i][3]));
      if (!__any(m == ~0u)) break;
    }
    ST(9);
#pragma unroll
    for (int i = 0; i < 4; ++i) lds_w128(l0 + L_OS + (4 * w + i) * 1024 + vk, v[i]);
  }
  BAR();
  ST(10);
  if (w < 4) {
    f32x16 a;
    asm volatile(
      "ds_read_b128 v[20:23], %[b] offset:0\n\t"
      "ds_read_b128 v[24:27], %[b] offset:1024\n\t"
      "ds_read_b128 v[28:31], %[b] offset:2048\n\t"
      "ds_read_b128 v[32:35], %[b] offset:3072\n\t"
      "ds_read_b128 v[36:39], %[b] offset:4096\n\t"
      "ds_read_b128 v[40:43], %[b] offset:5120\n\t"
      "ds_read_b128 v[44:47], %[b] offset:6144\n\t"
      "ds_read_b128 v[48:51], %[b] offset:7168\n\t"
      "s_waitcnt lgkmcnt(7)\n\t"
      "v_mfma_f32_32x32x16_bf16 v[68:83], a[0:3], v[20:23], 0\n\t"
      "ds_read_b128 v[20:23], %[b] offset:8192\n\t"
      "ds_read_b64_tr_b16 a[0:1], %[a1] offset:16384\n\t"
      "ds_read_b64_tr_b16 a[2:3], %[a1] offset:18432\n\t"
      "s_waitcnt lgkmcnt(9)\n\t"
      "v_mfma_f32_32x32x16_bf16 v[68:83], a[4:7], v[24:27], v[68:83]\n\t"
      "ds_read_b128 v[24:27], %[b] offset:9216\n\t"
      "ds_read_b64_tr_b16 a[4:5], %[a1] offset:20480\n\t"
      "ds_read_b64_tr_b16 a[6:7], %[a1] offset:22528\n\t"
      "s_waitcnt lgkmcnt(11)\n\t"
      "v_mfma_f32_32x32x16_bf16 v[68:83], a[8:11], v[28:31], v[68:83]\n\t"
      "ds_read_b128 v[28:31], %[b] offset:10240\n\t"
      "ds_read_b64_tr_b16 a[8:9], %[a1] offset:24576\n\t"
      "ds_read_b64_tr_b16 a[10:11], %[a1] offset:26624\n\t"
      "s_waitcnt lgkmcnt(13)\n\t"
      "v_mfma_f32_32x32x16_bf16 v[68:83], a[12:15], v[32:35], v[68:83]\n\t"
      "ds_read_b128 v[32:35], %[b] offset:11264\n\t"
      "ds_read_b64_tr_b16 a[12:13], %[a1] offset:28672\n\t"
      "ds_read_b64_tr_b16 a[14:15], %[a1] offset:30720\n\t"
      "s_waitcnt lgkmcnt(15)\n\t"
      "v_mfma_f32_32x32x16_bf16 v[68:83], a[16:19], v[36:39], v[68:83]\n\t"
      "ds_read_b128 v[36:39], %[b] offset:12288\n\t"
      "ds_read_b64_tr_b16 a[16:17], %[a1] offset:32768\n\t"
      "ds_read_b64_tr_b16 a[18:19], %[a1] offset:34816\n\t"
      "s_waitcnt lgkmcnt(15)\n\t"
      "v_mfma_f32_32x32x16_bf16 v[68:83], a[20:23], v[40:43], v[68:83]\n\t"
      "ds_read_b128 v[40:43], %[b] offset:13312\n\t"
      "ds_read_b64_tr_b16 a[20:21], %[a1] offset:36864\n\t"
      "ds_read_b64_tr_b16 a[22:23], %[a1] offset:38912\n\t"
      "s_waitcnt lgkmcnt(15)\n\t"
      "v_mfma_f32_32x32x16_bf16 v[68:83], a[24:27], v[44:47], v[68:83]\n\t"
      "ds_read_b128 v[44:47], %[b] offset:14336\n\t"
      "ds_read_b64_tr_b16 a[24:25], %[a1] offset:40960\n\t"
      "ds_read_b64_tr_b16 a[26:27], %[a1] offset:43008\n\t"
      "s_waitcnt lgkmcnt(15)\n\t"
      "v_mfma_f32_32x32x16_bf16 v[68:83], a[28:31], v[48:51], v[68:83]\n\t"
      "ds_read_b128 v[48:51], %[b] offset:15360\n\t"
      "ds_read_b64_tr_b16 a[28:29], %[a1] offset:45056\n\t"
      "ds_read_b64_tr_b16 a[30:31], %[a1] offset:47104\n\t"
      "s_waitcnt lgkmcnt(15)\n\t"
      "v_mfma_f32_32x32x16_bf16 v[68:83], a[32:35], v[20:23], v[68:83]\n\t"
      "ds_read_b128 v[20:23], %[b] offset:16384\n\t"
      "ds_read_b64_tr_b16 a[32:33], %[a1] offset:49152\n\t"
      "ds_read_b64_tr_b16 a[34:35], %[a1] offset:51200\n\t"
      "s_waitcnt lgkmcnt(15)\n\t"
      "v_mfma_f32_32x32x16_bf16 v[68:83], a[36:39], v[24:27], v[68:83]\n\t"
      "ds_read_b128 v[24:27], %[b] offset:17408\n\t"
      "ds_read_b64_tr_b16 a[36:37], %[a1] offset:53248\n\t"
      "ds_read_b64_tr_b16 a[38:39], %[a1] offset:55296\n\t"
      "s_waitcnt lgkmcnt(15)\n\t"
      "v_mfma_f32_32x32x16_bf16 v[68:83], a[40:43], v[28:31], v[68:83]\n\t"
      "ds_read_b128 v[28:31], %[b] offset:18432\n\t"
      "ds_read_b64_tr_b16 a[40:41], %[a1] offset:57344\n\t"
      "ds_read_b64_tr_b16 a[42:43], %[a1] offset:59392\n\t"
      "s_waitcnt lgkmcnt(15)\n\t"
      "v_mfma_f32_32x32x16_bf16 v[68:83], a[44:47], v[32:35], v[68:83]\n\t"
      "ds_read_b128 v[32:35], %[b] offset:19456\n\t"
      "ds_read_b64_tr_b16 a[44:45], %[a1] offset:61440\n\t"
      "ds_read_b64_tr_b16 a[46:47], %[a1] offset:63488\n\t"
      "s_waitcnt lgkmcnt(15)\n\t"
      "v_mfma_f32_32x32x16_bf16 v[68:83], a[48:51], v[36:39], v[68:83]\n\t"
      "ds_read_b128 v[36:39], %[b] offset:20480\n\t"
      "s_waitcnt lgkmcnt(15)\n\t"
      "v_mfma_f32_32x32x16_bf16 v[68:83], a[52:55], v[40:43], v[68:83]\n\t"
      "ds_read_b128 v[40:43], %[b] offset:21504\n\t"
      "s_waitcnt lgkmcnt(15)\n\t"
      "v_mfma_f32_32x32x16_bf16 v[68:83], a[56:59], v[44:47], v[68:83]\n\t"
      "ds_read_b128 v[44:47], %[b] offset:22528\n\t"
      "s_waitcnt lgkmcnt(15)\n\t"
      "v_mfma_f32_32x32x16_bf16 v[68:83], a[60:63], v[48:51], v[68:83]\n\t"
      "ds_read_b128 v[48:51], %[b] offset:23552\n\t"
      "s_waitcnt lgkmcnt(15)\n\t"
      "v_mfma_f32_32x32x16_bf16 v[68:83], a[64:67], v[20:23], v[68:83]\n\t"
      "ds_read_b128 v[20:23], %[b] offset:24576\n\t"
      "s_waitcnt lgkmcnt(13)\n\t"
      "v_mfma_f32_32x32x16_bf16 v[68:83], a[68:71], v[24:27], v[68:83]\n\t"
      "ds_read_b128 v[24:27], %[b] offset:25600\n\t"
      "s_waitcnt lgkmcnt(11)\n\t"
      "v_mfma_f32_32x32x16_bf16 v[68:83], a[72:75], v[28:31], v[68:83]\n\t"
      "ds_read_b128 v[28:31], %[b] offset:26624\n\t"
      "s_waitcnt lgkmcnt(9)\n\t"
      "v_mfma_f32_32x32x16_bf16 v[68:83], a[76:79], v[32:35], v[68:83]\n\t"
      "ds_read_b128 v[32:35], %[b] offset:27648\n\t"
      "s_waitcnt lgkmcnt(7)\n\t"
      "v_mfma_f32_32x32x16_bf16 v[68:83], a[0:3], v[36:39], v[68:83]\n\t"
      "ds_read_b128 v[36:39], %[b] offset:28672\n\t"
      "s_waitcnt lgkmcnt(7)\n\t"
      "v_mfma_f32_32x32x16_bf16 v[68:83], a[4:7], v[40:43], v[68:83]\n\t"
      "ds_read_b128 v[40:43], %[b] offset:29696\n\t"
      "s_waitcnt lgkmcnt(7)\n\t"
      "v_mfma_f32_32x32x16_bf16 v[68:83], a[8:11], v[44:47], v[68:83]\n\t"
      "ds_read_b128 v[44:47], %[b] offset:30720\n\t"
      "s_waitcnt lgkmcnt(7)\n\t"
      "v_mfma_f32_32x32x16_bf16 v[68:83], a[12:15], v[48:51], v[68:83]\n\t"
      "ds_read_b128 v[48:51], %[b] offset:31744\n\t"
      "s_waitcnt lgkmcnt(7)\n\t"
      "v_mfma_f32_32x32x16_bf16 v[68:83], a[16:19], v[20:23], v[68:83]\n\t"
      "s_waitcnt lgkmcnt(6)\n\t"
      "v_mfma_f32_32x32x16_bf16 v[68:83], a[20:23], v[24:27], v[68:83]\n\t"
      "s_waitcnt lgkmcnt(5)\n\t"
      "v_mfma_f32_32x32x16_bf16 v[68:83], a[24:27], v[28:31], v[68:83]\n\t"
      "s_waitcnt lgkmcnt(4)\n\t"
      "v_mfma_f32_32x32x16_bf16 v[68:83], a[28:31], v[32:35], v[68:83]\n\t"
      "s_waitcnt lgkmcnt(3)\n\t"
      "v_mfma_f32_32x32x16_bf16 v[68:83], a[32:35], v[36:39], v[68:83]\n\t"
      "s_waitcnt lgkmcnt(2)\n\t"
      "v_mfma_f32_32x32x16_bf16 v[68:83], a[36:39], v[40:43], v[68:83]\n\t"
      "s_waitcnt lgkmcnt(1)\n\t"
      "v_mfma_f32_32x32x16_bf16 v[68:83], a[40:43], v[44:47], v[68:83]\n\t"
      "s_waitcnt lgkmcnt(0)\n\t"
      "v_mfma_f32_32x32x16_bf16 v[68:83], a[44:47], v[48:51], v[68:83]\n\t"
      "s_nop 15\n\t"
      "s_nop 3\n\t"
      : "=&{v[68:83]}"(a) : [b] "v"(L_OS + vk), [a1] "v"(abase + 65536) : "memory", ACLOB, "v20","v21","v22","v23","v24","v25","v26","v27","v28","v29","v30","v31","v32","v33","v34","v35","v36","v37","v38","v39","v40","v41","v42","v43","v44","v45","v46","v47","v48","v49","v50","v51");
    float r[16];
#pragma unroll
    for (int i = 0; i < 16; ++i) r[i] = fmaf(gate, a[i], tb[i]);
    *(u32x4*)(out + (size_t)token * D_ + col) = u32x4{cvtpk(r[0], r[1]), cvtpk(r[2], r[3]), cvtpk(r[4], r[5]), cvtpk(r[6], r[7])};
    *(u32x4*)(out + (size_t)token * D_ + col + 8) = u32x4{cvtpk(r[8], r[9]), cvtpk(r[10], r[11]), cvtpk(r[12], r[13]), cvtpk(r[14], r[15])};
    if (b == 0 && w == 0 && lane == 0) ST64((u32x2{tlo, thi}), WS_EP);
  }
  ST(11);
}

__global__ void __launch_bounds__(576, 3)
fused(const u16* __restrict__ x, const u16* __restrict__ W, const u16* __restrict__ wo, const u16* __restrict__ wr,
      const u16* __restrict__ wg, const u16* __restrict__ wu, const u16* __restrict__ wd, const u16* __restrict__ gw,
      char* __restrict__ ws, u16* __restrict__ out) {
  Stamps stp; asm volatile("s_memrealtime %0\n\ts_waitcnt lgkmcnt(0)" : "=s"(stp.t0) :: "memory"); stp.t = stp.t0;
  constexpr unsigned l0 = 0;
  const int b = blockIdx.x;
  int hg, rb; MAPB(b, hg, rb);
  const int lane = threadIdx.x & 63, w = __builtin_amdgcn_readfirstlane(threadIdx.x >> 6);
  const int c31 = lane & 31, hh = lane >> 5, blk = (lane >> 4) & 1, q4 = (lane & 15) >> 2, p4 = lane & 3;
  const int tt = 2 * rb + (hg >> 2), cg = hg & 3;
  const __amdgpu_buffer_rsrc_t rs = __builtin_amdgcn_make_buffer_rsrc(ws, 0, (int)WS_END, 0x00020000);
  STDECL
  ST(0);
  asm volatile("" :: "s"(x), "s"(W), "s"(wr), "s"(gw), "s"(ws));
#define GET_TAG() u64 ep_; asm volatile("s_load_dwordx2 %0, %1, 0x0\n\ts_waitcnt lgkmcnt(0)" : "=s"(ep_) : "s"(ws + WS_EP) : "memory"); \
  const u64 tag = ep_ * 0x9E3779B97F4A7C15ull + 0xD1B54A32D192ED03ull; const u32 thi = (u32)(tag >> 32), tlo = (u32)tag

  if (w == 8) {
#pragma unroll
    for (int i = 0; i < 8; ++i) ST128((u32x4{~0u, ~0u, ~0u, ~0u}), WS_OF + ((2 * rb + (i >> 2)) * 32 + 4 * hg + (i & 3)) * 1024 + lane * 16);
    if (lane < 4) LDSU(L_CTL + lane * 4) = 0;
    GET_TAG();
#pragma unroll 1
    for (int i = 0; i < 6; ++i) BAR();
    DRAIN();
    if (lane == 0) ST64((u32x2{tlo, thi}), WS_OL + (rb * 8 + hg) * 128);
#pragma unroll 1
    for (int i = 0; i < 5; ++i) BAR();
    const int cg1 = (b & 7) * 28 + (b >> 3);
    const int ks2 = b & 7, cg2 = b >> 3;
    const bool has1 = (b >> 3) < 28;
    ST(2);
    __builtin_amdgcn_s_setprio(HPRIO);
    const rsrc_t ro = mkrs(wo, D_ * D_ * 2);
    const unsigned vwo = (lane >> 4) * (D_ * 2) + cg * 256 + (((lane & 15) ^ ((lane >> 4) << 2)) * 16);
    unsigned key = 0;
    for (int spin = 0; spin < SPIN_MAX; ++spin) {
      if (spin) __builtin_amdgcn_s_sleep(2);
      FRESH();
      const u32x2 f = LD64(WS_FL + (hg * 32 + ((lane + rb) & 31)) * 128);
      key = f[0] & 0x7ffffffu;
      const unsigned rdy = (unsigned)__ballot(f[1] == thi && (f[0] ^ tlo) < 0x8000000u);
      if (lane == 0) LDSU(L_CTL) = rdy;
      if (rdy == ~0u) break;
    }
    if (lane == 0) LDSU(L_CTL) = ~0u;
    ST(3);
    const unsigned best = wave_max_u32(key);
    const int sel = 2047 - (int)(best & 2047u);
    if (lane == 0) LDSU(L_CTL + 12) = best | 0x80000000u;
    if (has1) {
      const rsrc_t rg = mkrs(wg, D_ * F_ * 2), ru = mkrs(wu, D_ * F_ * 2);
      const unsigned vo = (lane >> 1) * (F_ * 2) + cg1 * 16;
#pragma unroll 1
      for (int i = 0; i < 16; ++i)
        asm volatile("s_mov_b32 m0, %0\n\ts_mov_b64 exec, %5\n\tbuffer_load_dwordx4 %1, %2, %4 offen lds\n\ts_not_b64 exec, exec\n\t"
                     "buffer_load_dwordx4 %1, %3, %4 offen lds\n\ts_mov_b64 exec, -1"
                     :: "s"(L_GU + i * 1024), "v"(vo), "s"(rg), "s"(ru), "s"(i * 32 * F_ * 2), "s"(0x5555555555555555ull) : "memory", "scc");
    }
    {
      const rsrc_t rd = mkrs(wd, D_ * F_ * 2);
      const unsigned vo = (ks2 * 224 + (lane >> 1)) * (D_ * 2) + cg2 * 32 + (lane & 1) * 16;
#pragma unroll 1
      for (int i = 0; i < 7; ++i) dma<L_WD>(vo, rd, i * 32 * D_ * 2, i * 1024);
    }
    if (has1) {
      dma<L_XS>(lane * 16, mkrs(x, 2048 * D_ * 2), sel * (D_ * 2), 0);
      ST(4);
      DRAIN();
      ST(5);
      f32x4 d = {0};
      const unsigned ba = l0 + L_GU + (8 * (lane >> 4) + q4) * 32 + p4 * 8;
#pragma unroll 4
      for (int u = 0; u < 16; ++u)
        d = MFMA16(lds_b128(l0 + L_XS + (4 * u + (lane >> 4)) * 16), cat8(lds_tr(ba + u * 1024), lds_tr(ba + u * 1024 + 128)), d);
      const float gp = bfround(d[0]), up = bfround(__shfl_down(d[0], 8));
      const unsigned hb = cvtpk(bfround(gp * sigmoidf_(gp)) * up, 0.f) & 0xffffu;
      if (lane < 8) ST64((u32x2{(tlo & 0xffff0000u) | hb, thi}), WS_H + (cg1 * 8 + lane) * 8);
    }
    ST(6);
#pragma unroll 1
    for (int i = 25; i < 80; ++i) dma<0>(vwo, ro, i * 4096, i * 1024);
    ST(7);
    for (int spin = 0; spin < SPIN_MAX; ++spin) {
      if (spin) __builtin_amdgcn_s_sleep(4);
      FRESH();
      const unsigned o = WS_H + ks2 * 1792 + (lane < 56 ? lane : 0) * 32;
      const u32x4 a = LD128(o), c = LD128(o + 16);
      const bool ok = a[1] == thi && a[3] == thi && c[1] == thi && c[3] == thi &&
                      ((a[0] ^ tlo) | (a[2] ^ tlo) | (c[0] ^ tlo) | (c[2] ^ tlo)) < 0x10000u;
      *(LAS u32x2*)(size_t)(l0 + L_HS + lane * 8) = u32x2{(a[0] & 0xffffu) | (a[2] << 16), (c[0] & 0xffffu) | (c[2] << 16)};
      if (__all(ok)) break;
    }
    ST(8);
    {
      f32x4 d = {0};
      const unsigned ba = l0 + L_WD + (8 * (lane >> 4) + q4) * 32 + p4 * 8;
#pragma unroll
      for (int u = 0; u < 7; ++u)
        d = MFMA16(lds_b128(l0 + L_HS + (4 * u + (lane >> 4)) * 16), cat8(lds_tr(ba + u * 1024), lds_tr(ba + u * 1024 + 128)), d);
      const unsigned yb = __float_as_uint(d[0]);
      if (lane < 16) ST128((u32x4{(tlo & 0xffff0000u) | (yb & 0xffffu), thi, (tlo & 0xffff0000u) | (yb >> 16), thi}),
                           WS_Y + (ks2 * 512 + cg2 * 16 + lane) * 16);
      asm volatile("s_waitcnt lgkmcnt(0)" ::: "memory");
    }
    ST(9);
#pragma unroll 1
    for (int i = 80; i < 128 + 25; ++i) { const int r = i & 127; dma<0>(vwo, ro, r * 4096, r * 1024); }
    for (int spin = 0; spin < SPIN_MAX; ++spin) {
      if (spin) __builtin_amdgcn_s_sleep(4);
      FRESH();
      const u32x2 f = LD64(WS_OL + (rb * 8 + (lane & 7)) * 128);
      if (__all(f[0] == tlo && f[1] == thi)) break;
    }
    ST(1);
    WAIT_BAR(0);
    BAR();
    STDUMP();
    return;
  }

  const int rot = (rb + hg) & 7;
  const rsrc_t rx = mkrs(x, 2048 * D_ * 2), rw = mkrs(W, D_ * 1536 * 2);
  const unsigned mw = w * 1024;
  const unsigned vx = (rb * 64 + w * 8 + (lane >> 3)) * 1024 + (((lane & 7) ^ ((w * 4 + (lane >> 4)) & 7)) << 4);
  const unsigned vq = ((lane >> 3) + w * 8) * 3072 + hg * 128 + (((lane & 7) ^ (((lane >> 4) & 1) << 2)) << 4);
  const unsigned vkv = vq + (w < 4 ? 1024 : 2048 - 32 * 3072);
  if (w == 4) dma<L_WV>(((lane + 8 * rot) & 63) * 16, mkrs(wr, 1024), 0, 0);
  if (w == 5) dma<L_WV + 1024>(((lane + 8 * rot) & 63) * 16, mkrs(gw, 1024), 0, 0);
#define KBLK(g) ((unsigned)(((g) + rot) & 7))
#define ISSX(g) dma<L_X + (g) * 8192>(vx, rx, KBLK(g) * 128, mw)
#define ISSUE(r) do { if ((r) < 16) dma<L_W + (((r) + 4) % 10) * 8192>(vkv, rw, KBLK((r) >> 1) * 196608 + ((r) & 1) * 98304, mw); \
                      else dma<L_W + (((r) + 4) % 10) * 8192>(vq, rw, KBLK(r) * 196608, mw); } while (0)
#define ISSG(g) do { ISSX(g); ISSUE(2 * (g)); ISSUE(2 * (g) + 1); } while (0)
#define LEAD 4
  ISSG(0); ISSG(1); ISSG(2);
  if (LEAD == 4) ISSG(3);

  const int part = w >> 2, pair = w & 1, tile = (w >> 1) & 1;
  unsigned xa0 = L_X + (tile * 32 + c31) * 128 + ((hh ^ ((c31 >> 1) & 7)) << 4);
  unsigned xa1 = xa0 ^ 32, xa2 = xa0 ^ 64, xa3 = xa0 ^ 96;
  unsigned wq0 = L_W + (8 * hh + q4) * 128 + (((4 * pair + 2 * blk + (p4 >> 1)) ^ (((q4 >> 1) & 1) << 2)) << 4) + 8 * (p4 & 1);
  unsigned wa0 = wq0 + part * 4096, wa1 = wa0 + 65536;
  asm volatile("" : "+v"(xa0), "+v"(xa1), "+v"(xa2), "+v"(xa3), "+v"(wa0), "+v"(wa1), "+v"(wq0));
  f32x16 acc = {0};
  GET_TAG();
  {
    bf16x8 wf[2][4], xf[2][4];
#define WADDR(g, u) ((((2 * (g) + 4 + ((u) >> 1)) % 10) >= 8 ? wa1 - 65536 : wa0) + ((2 * (g) + 4 + ((u) >> 1)) % 10) * 8192 + ((u) & 1) * 2048)
#define RD1(g, u, xa) do { xf[(g) & 1][u] = lds_b128(xa + (g) * 8192); \
      wf[(g) & 1][u] = cat8(lds_tr(WADDR(g, u)), lds_tr(WADDR(g, u) + 512)); } while (0)
#define RD4(g) do { RD1(g, 0, xa0); RD1(g, 1, xa1); RD1(g, 2, xa2); RD1(g, 3, xa3); } while (0)
#define MM(V, g, u) do { if (V) acc = MFMA32(xf[(g) & 1][u], wf[(g) & 1][u], acc); else acc = MFMA32(wf[(g) & 1][u], xf[(g) & 1][u], acc); SBAR(); } while (0)
#define NXT(g, i) do { if ((g) + LEAD <= 7) { if ((i) == 0) ISSX(((g) + LEAD) & 7); else ISSUE(2 * ((g) + LEAD) + (i) - 1); } \
      if ((g) >= 4 && (i) > 0) ISSUE(2 * (g) + 7 + (i)); } while (0)
#define GROUP(V, g, N) do { WAIT_BAR(N); STG(4 + (g)); MM(V, (g) - 1, 0); RD1(g, 0, xa0); NXT(g, 0); SBAR(); MM(V, (g) - 1, 1); RD1(g, 1, xa1); NXT(g, 1); SBAR(); \
      MM(V, (g) - 1, 2); RD1(g, 2, xa2); NXT(g, 2); SBAR(); MM(V, (g) - 1, 3); RD1(g, 3, xa3); SBAR(); } while (0)
#define WB0 9
#define KVLOOP_(V, N) do { WAIT_BAR(N); ST(1); RD4(0); NXT(0, 0); NXT(0, 1); NXT(0, 2); SBAR(); GROUP(V, 1, N); GROUP(V, 2, N); GROUP(V, 3, N); GROUP(V, 4, N); \
      GROUP(V, 5, 8); GROUP(V, 6, 7); if (LEAD == 4) GROUP(V, 7, 6); else GROUP(V, 7, 5); MM(V, 7, 0); MM(V, 7, 1); MM(V, 7, 2); MM(V, 7, 3); } while (0)
#define KVLOOP__(V, N) KVLOOP_(V, N)
#define KVLOOP(V) KVLOOP__(V, WB0)
    if (part == 0) KVLOOP(false); else KVLOOP(true);
  }
  ST(2);
  const u32x4 p0 = pack8(acc, 0), p1 = pack8(acc, 1);
  unsigned o0, o1;
  int h0, h1;
  if (part == 0) {
    const int s23 = (c31 & 19) | ((c31 & 4) << 1) | ((c31 & 8) >> 1);
    o0 = o1 = (s23 + 32 * hh) * 16; h0 = 2 * pair; h1 = 2 * pair + 1;
  } else {
    o0 = 1024 + (hh * 16 + (lane & 15)) * 16; o1 = o0 + 512; h0 = h1 = 2 * pair + blk;
  }
  ST128(p0, WS_KV + (((4 * hg + h0) * 2 + tile) * 32 + rb) * KVB + o0);
  ST128(p1, WS_KV + (((4 * hg + h1) * 2 + tile) * 32 + rb) * KVB + o1);
  if (part == 1 && lane < 2) ST128((u32x4{0x3F803F80u, 0x3F803F80u, 0x3F803F80u, 0x3F803F80u}), WS_KV + (((4 * hg + 2 * pair + lane) * 2 + tile) * 32 + rb) * KVB + 2048);
  if (part == 0) WAIT_BAR(2); else WAIT_BAR(3);
  ST(13);
  lds_w128(l0 + L_KV + (h0 * 2 + tile) * 2048 + o0, p0);
  lds_w128(l0 + L_KV + (h1 * 2 + tile) * 2048 + o1, p1);
  if (part == 0) {
    f32x16 qacc = {0};
    bf16x8 wf[2][4], xf[2][4];
#define QR1(r, u, xa) do { xf[(r) & 1][u] = lds_b128(xa + (r) * 8192); \
      wf[(r) & 1][u] = cat8(lds_tr(wq0 + (r) * 8192 + (u) * 2048), lds_tr(wq0 + (r) * 8192 + (u) * 2048 + 512)); } while (0)
#define QR4(r) do { QR1(r, 0, xa0); QR1(r, 1, xa1); QR1(r, 2, xa2); QR1(r, 3, xa3); } while (0)
    QR4(0);
    SBAR();
#define QS(r, u, xa) do { qacc = MFMA32(wf[(r) & 1][u], xf[(r) & 1][u], qacc); SBAR(); if ((r) < 7) QR1((r) + 1, u, xa); SBAR(); } while (0)
#define QG(r) do { QS(r, 0, xa0); QS(r, 1, xa1); QS(r, 2, xa2); QS(r, 3, xa3); } while (0)
    QG(0); QG(1); QG(2); QG(3); QG(4); QG(5); QG(6); QG(7);
    const float qs = 0.25f * 1.4426950408889634f;
    lds_w128(l0 + L_QX + ((2 * pair) * 2 + tile) * 1024 + lane * 16, pack8(qacc, 0, qs));
    lds_w128(l0 + L_QX + ((2 * pair + 1) * 2 + tile) * 1024 + lane * 16, pack8(qacc, 1, qs));
  } else {
    const int tok = 16 * (w & 3) + (lane & 15), g4 = lane >> 4;
    f32x4 d = {0};
    unsigned fa0 = L_WV + (lane & 1) * 1024 + g4 * 16, fb0 = L_X + tok * 128 + ((g4 ^ ((tok >> 1) & 7)) << 4), fb1 = fb0 ^ 64;
    asm volatile("" : "+v"(fa0), "+v"(fb0), "+v"(fb1));
    bf16x8 fa[16], fb[16];
#define RGR(u) do { fa[u] = lds_b128(fa0 + (u) * 64); fb[u] = lds_b128((((u) & 1) ? fb1 : fb0) + ((u) >> 1) * 8192); } while (0)
    RGR(0); RGR(1); RGR(2); RGR(3);
    SBAR();
#pragma unroll
    for (int u = 0; u < 16; ++u) { d = MFMA16(fa[u], fb[u], d); SBAR(); if (u + 4 < 16) RGR((u + 4) & 15); SBAR(); }
    const unsigned r16 = cvtpk(d[0], 0.f) & 0xffffu;
    unsigned key = (((r16 & 0x8000u) ? (~r16 & 0xffffu) : (r16 | 0x8000u)) << 11) | (2047 - (rb * 64 + tok));
    if (lane < 16) {
      asm volatile("ds_max_u32 %0, %1" :: "v"(L_CTL + 8), "v"(key) : "memory");
      *(LAS float*)(size_t)(l0 + L_RG + tok * 4) = bfround(sigmoidf_(bfround(d[1])));
    }
    if (w == 7 && lane < 2) { const unsigned v = lane ? 0u : 0x3F803F80u; lds_w128(l0 + L_ONE + lane * 16, u32x4{v, v, v, v}); }
  }
  ST(3);
  BAR();
  DRAIN();
  if (lane == 0 && __hip_atomic_fetch_add((LAS u32*)(size_t)(L_CTL + 4), 1u, __ATOMIC_RELAXED, __HIP_MEMORY_SCOPE_WORKGROUP) == 7)
    ST64((u32x2{(tlo & 0xf8000000u) | LDSU(L_CTL + 8), thi}), WS_FL + (hg * 32 + rb) * 128);
  ST(14);

  const int j = w & 3, kh = w >> 2, head = 4 * hg + j;
  const bool onerow = c31 == 16 || c31 == 20;
  const unsigned idx = 0xDDDDDDDDu, vk = lane * 16;
  const unsigned vv0 = c31 < 16 ? 1024 + (hh * 16 + c31) * 16 : (onerow ? 2048u : 0x7f000000u);
  const unsigned vv1 = c31 < 16 ? vv0 + 512 : vv0;
  const bf16x8 qa = lds_b128(l0 + L_QX + (j * 2) * 1024 + lane * 16), qb = lds_b128(l0 + L_QX + (j * 2 + 1) * 1024 + lane * 16);
  const float gate = *(const LAS float*)(size_t)(l0 + L_RG + ((hg >> 2) * 32 + c31) * 4);
  f32x16 sa, sb, mm;
  {
    const unsigned lk = l0 + L_KV + (j * 2 + kh) * 2048;
    const unsigned la = lk + vk, lm = l0 + L_KV + j * 4096 + vk;
    const unsigned lv0 = c31 < 16 ? lk + vv0 : l0 + L_ONE + (onerow ? 0 : 16), lv1 = c31 < 16 ? lv0 + 512 : lv0;
#define Z1(r) "v_accvgpr_write_b32 a[" STR(r) "], 0\n\t"
#define Z4(r) Z1(r) Z1(r+1) Z1(r+2) Z1(r+3)
#define Z16(r) Z4(r) Z4(r+4) Z4(r+8) Z4(r+12)
    asm volatile("ds_read_b128 " KS(1) ", %[lm]\n\tds_read_b128 " KS(0) ", %[la]\n\tds_read_b128 " V0(0) ", %[a0]\n\tds_read_b128 " V1(0) ", %[a1]\n\t"
                 Z16(0) Z16(16)
                 "s_waitcnt lgkmcnt(0)\n\ts_barrier\n\t"
                 "v_mfma_f32_32x32x16_bf16 v[36:51], " KS(1) ", %[qa], 0\n\t"
                 "v_mfma_f32_32x32x16_bf16 v[52:67], " KS(1) ", %[qb], 0\n\t"
                 "s_nop 15\n\ts_nop 3\n\t"
                 : "=&{v[36:51]}"(sa), "=&{v[52:67]}"(sb)
                 : [lm] "v"(lm), [la] "v"(la), [a0] "v"(lv0), [a1] "v"(lv1), [qa] "v"(qa), [qb] "v"(qb)
                 : "memory", ACLOB);
    const float nm = -halves_max(fmaxf(max16(sa), max16(sb)));
#pragma unroll
    for (int i = 0; i < 16; ++i) mm[i] = nm;
    asm volatile("s_nop 1\n\t" QKA(0) QKB(0) "s_nop 11\n\t"
                 EX4(36) EX4(40) EX4(44) SMF(OA, V0(0), 36) EX4(48) EX4(52) SMF(OA, V1(0), 44) EX4(56) EX4(60) SMF(OB, V0(0), 52) EX4(64)
                 : "=&{v[36:51]}"(sa), "=&{v[52:67]}"(sb)
                 : "{v[68:83]}"(mm), [qa] "v"(qa), [qb] "v"(qb), [idx] "v"(idx) : "memory", ACLOB);
  }
  ST(4);
  ST(12);
  const unsigned kvb = WS_KV + (head * 2 + kh) * 32 * KVB;
  const unsigned tbl = kvb + ((lane + rb) & 31) * KVB;
  unsigned todo = 0xfffffffeu, ready = 0;
  int spins = 0;
  auto pick = [&]() -> unsigned {
    while (!(ready & todo)) {
      if (spins) __builtin_amdgcn_s_sleep(1);
      ready = __builtin_amdgcn_readfirstlane(LDSU(L_CTL));
      if (++spins > (SPIN_MAX << 4)) ready = ~0u;
    }
    const int i = __builtin_ctz(ready & todo);
    todo &= ~(1u << i);
    return __builtin_amdgcn_readlane(tbl, i);
  };
  unsigned s3, s4, s5, s6, s7;
  {
    const unsigned s1 = pick(), s2 = pick();
    s3 = pick(); s4 = pick();
    ST(5);
    asm volatile("s_nop 4\n\t" LDK(1, "%[s1]") LDV(1, "%[s1]") LDK(2, "%[s2]") LDV(2, "%[s2]") LDK(3, "%[s3]") LDV(3, "%[s3]") LDK(0, "%[s4]")
                 "s_waitcnt vmcnt(9)\n\t" QKA(1) "s_nop 15\n\t"
                 : "=&{v[36:51]}"(sa)
                 : "{v[68:83]}"(mm), [qa] "v"(qa), [vk] "v"(vk), [v0] "v"(vv0), [v1] "v"(vv1), [rs] "s"(rs),
                   [s1] "s"(s1), [s2] "s"(s2), [s3] "s"(s3), [s4] "s"(s4)
                 : "memory", ACLOB);
  }
  ST(6);
  __builtin_amdgcn_s_setprio(2);
  s5 = pick(); TILE(1, 0, 2, s4, s5);
  s6 = pick(); TILE(2, 1, 3, s5, s6);
  s7 = pick(); TILE(3, 2, 0, s6, s7);
#pragma unroll 1
  for (int t = 4; t < 28; t += 4) {
    if (t == 20) __builtin_amdgcn_s_setprio(1);
    s4 = pick(); TILE(0, 3, 1, s7, s4);
    s5 = pick(); TILE(1, 0, 2, s4, s5);
    s6 = pick(); TILE(2, 1, 3, s5, s6);
    s7 = pick(); TILE(3, 2, 0, s6, s7);
  }
  TILE(0, 3, 1, s7, s7);
  TILE(1, 0, 2, s7, s7);
  __builtin_amdgcn_s_setprio(0);
  TILE(2, 1, 3, s7, s7);
  TILE(3, 2, 0, s7, s7);
  float oa[9], ob[9];
#define RD(i, r) "v_accvgpr_read_b32 %" STR(i) ", a" STR(r) "\n\t"
  asm volatile("s_nop 1\n\t" SMF(OB, V1(3), 60) "s_waitcnt vmcnt(0)\n\ts_nop 15\n\ts_nop 3\n\t"
               RD(0, 0) RD(1, 1) RD(2, 2) RD(3, 3) RD(4, 4) RD(5, 5) RD(6, 6) RD(7, 7) RD(8, 8)
               RD(9, 16) RD(10, 17) RD(11, 18) RD(12, 19) RD(13, 20) RD(14, 21) RD(15, 22) RD(16, 23) RD(17, 24)
               : "=v"(oa[0]), "=v"(oa[1]), "=v"(oa[2]), "=v"(oa[3]), "=v"(oa[4]), "=v"(oa[5]), "=v"(oa[6]), "=v"(oa[7]), "=v"(oa[8]),
                 "=v"(ob[0]), "=v"(ob[1]), "=v"(ob[2]), "=v"(ob[3]), "=v"(ob[4]), "=v"(ob[5]), "=v"(ob[6]), "=v"(ob[7]), "=v"(ob[8])
               : "{v[52:67]}"(sb), [idx] "v"(idx) : "memory", ACLOB);
  ST(7);
  TAIL(ws, x, out, rs, b, w, oa, ob, qa, qb, vk, vv0, vv1, idx, gate, tlo, thi STPASS);
  STDUMP();
}

extern "C" void kernel_launch(void* const* d_in, const int* in_sizes, int n_in, void* d_out, int out_size, void* d_ws, size_t ws_size,
                              hipStream_t stream) {
  (void)hipFuncSetAttribute((const void*)fused, hipFuncAttributeMaxDynamicSharedMemorySize, LDS_BYTES);
  fused<<<256, 576, LDS_BYTES, stream>>>((const u16*)d_in[0], (const u16*)d_in[1], (const u16*)d_in[2], (const u16*)d_in[4],
                                         (const u16*)d_in[5], (const u16*)d_in[6], (const u16*)d_in[7], (const u16*)d_in[8],
                                         (char*)d_ws, (u16*)d_out);
}
```

```cpp
#define DIAG 99
#define DIAGW 1
#include <hip/hip_runtime.h>
#include <hip/hip_bf16.h>

typedef unsigned short u16;
typedef unsigned int u32;
typedef unsigned long long u64;
typedef __attribute__((ext_vector_type(8))) __bf16 bf16x8;
typedef __attribute__((ext_vector_type(16))) __bf16 bf16x16;
typedef __attribute__((ext_vector_type(16))) float f32x16;
typedef __attribute__((ext_vector_type(8))) float f32x8;
typedef __attribute__((ext_vector_type(4))) float f32x4;
typedef __attribute__((ext_vector_type(4))) unsigned u32x4;
typedef __attribute__((ext_vector_type(2))) unsigned u32x2;
typedef __attribute__((ext_vector_type(4))) short s16x4;
#define LAS __attribute__((address_space(3)))

constexpr int D_ = 512, F_ = 1792;
constexpr u32 KVB = 2176;
constexpr u32 WS_KV = 0;
constexpr u32 WS_OF = 8u << 20;
constexpr u32 WS_FL = 10u << 20;
constexpr u32 WS_OL = WS_FL + 32768;
constexpr u32 WS_H  = WS_OL + 32768;
constexpr u32 WS_Y  = WS_H + 16384;
constexpr u32 WS_EP = WS_Y + 65536;
constexpr u32 WS_END = WS_EP + 128;
constexpr u32 L_X = 0, L_W = 65536, L_KV = 131072, L_WV = 147456, L_QX = 149504, L_RG = 157696, L_ONE = 158208;
constexpr u32 L_GU = 0, L_WD = 16384, L_XS = 23552, L_HS = 24576;
constexpr u32 L_OS = 131072;
constexpr u32 L_CTL = 163808;
constexpr int LDS_BYTES = 163840;
constexpr int SPIN_MAX = 1 << 18;

#define STG(i)
struct Stamps { u64 t0, t; };
#define ST(i) do { if ((i) == DIAG) { asm volatile("s_memrealtime %0\n\ts_waitcnt lgkmcnt(0)" : "=s"(stp.t) :: "memory"); } } while (0)
#define STDECL
#define STDUMP() do { if (blockIdx.x == 77 && (threadIdx.x >> 6) == DIAGW) { const u64 end = stp.t0 + 0 + 2 * (stp.t - stp.t0); while (__builtin_amdgcn_s_memrealtime() < end) __builtin_amdgcn_s_sleep(1); } } while (0)
#define STARG , Stamps& stp
#define STPASS , stp

#define HPRIO 3
#define MAP 3
#define MAPB(b, hg, rb) do { if (MAP == 3) { hg = 2 * (b & 3) + ((b >> 3) & 1); rb = 16 * ((b >> 2) & 1) + (b >> 4); } \
  else if (MAP == 2) { hg = 4 * (b & 1) + ((b >> 3) & 3); rb = 8 * ((b >> 1) & 3) + (b >> 5); } \
  else if (MAP == 1) { hg = b & 7; rb = b >> 3; } else { hg = (b >> 3) & 7; rb = 4 * (b & 7) + (b >> 6); } } while (0)
typedef __amdgpu_buffer_rsrc_t rsrc_t;
__device__ __forceinline__ rsrc_t mkrs(const void* p, unsigned bytes) { return __builtin_amdgcn_make_buffer_rsrc((void*)p, 0, (int)bytes, 0x00020000); }
template <unsigned ADD>
__device__ __forceinline__ void dma(unsigned voff, rsrc_t r, unsigned soff, unsigned m) {
  asm volatile("s_add_u32 m0, %0, %c4\n\ts_nop 0\n\tbuffer_load_dwordx4 %1, %2, %3 offen lds" :: "s"(m), "v"(voff), "s"(r), "s"(soff), "i"(ADD) : "memory", "scc");
}
__device__ __forceinline__ unsigned cvtpk(float lo, float hi) {
  typedef __attribute__((ext_vector_type(2))) __bf16 bf16x2;
  typedef __attribute__((ext_vector_type(2))) float f32x2;
  f32x2 v = {lo, hi};
  return __builtin_bit_cast(unsigned, __builtin_convertvector(v, bf16x2));
}
__device__ __forceinline__ float bfround(float f) { return __uint_as_float(cvtpk(f, 0.f) << 16); }
__device__ __forceinline__ float bflo(unsigned u) { return __uint_as_float(u << 16); }
__device__ __forceinline__ float bfhi(unsigned u) { return __uint_as_float(u & 0xffff0000u); }
__device__ __forceinline__ float sigmoidf_(float z) { return __builtin_amdgcn_rcpf(1.f + __expf(-z)); }
__device__ __forceinline__ bf16x8 lds_b128(unsigned a) { return *(const LAS bf16x8*)(size_t)a; }
__device__ __forceinline__ void lds_w128(unsigned a, u32x4 v) { *(LAS u32x4*)(size_t)a = v; }
__device__ __forceinline__ s16x4 lds_tr(unsigned a) { return __builtin_amdgcn_ds_read_tr16_b64_v4i16((LAS s16x4*)(size_t)a); }
__device__ __forceinline__ bf16x8 cat8(s16x4 a, s16x4 b) {
  typedef __attribute__((ext_vector_type(8))) short s16x8;
  s16x8 r = {a[0], a[1], a[2], a[3], b[0], b[1], b[2], b[3]};
  return __builtin_bit_cast(bf16x8, r);
}
__device__ __forceinline__ u32x4 pack8(const f32x16& a, int z, float s) {
  return u32x4{cvtpk(a[8 * z] * s, a[8 * z + 1] * s), cvtpk(a[8 * z + 2] * s, a[8 * z + 3] * s),
               cvtpk(a[8 * z + 4] * s, a[8 * z + 5] * s), cvtpk(a[8 * z + 6] * s, a[8 * z + 7] * s)};
}
__device__ __forceinline__ u32x4 pack8(const f32x16& a, int z) {
  return u32x4{cvtpk(a[8 * z], a[8 * z + 1]), cvtpk(a[8 * z + 2], a[8 * z + 3]), cvtpk(a[8 * z + 4], a[8 * z + 5]), cvtpk(a[8 * z + 6], a[8 * z + 7])};
}
__device__ __forceinline__ unsigned wave_max_u32(unsigned v) {
#pragma unroll
  for (int m = 32; m >= 1; m >>= 1) v = max(v, (unsigned)__shfl_xor((int)v, m));
  return __builtin_amdgcn_readfirstlane(v);
}
#define MFMA32(a, b, c) __builtin_amdgcn_mfma_f32_32x32x16_bf16(a, b, c, 0, 0, 0)
#define MFMA16(a, b, c) __builtin_amdgcn_mfma_f32_16x16x32_bf16(a, b, c, 0, 0, 0)
#define WAIT_BAR(N) asm volatile("s_waitcnt vmcnt(" #N ") lgkmcnt(0)\n\ts_barrier" ::: "memory")
#define BAR() asm volatile("s_waitcnt lgkmcnt(0)\n\ts_barrier" ::: "memory")
#define DRAIN() asm volatile("s_waitcnt vmcnt(0)" ::: "memory")
#define SBAR() __builtin_amdgcn_sched_barrier(0)
#define FRESH() asm volatile("" ::: "memory")
#define LDSU(a) (*(volatile LAS u32*)(size_t)(a))
#define LD128(off) __builtin_amdgcn_raw_buffer_load_b128(rs, (off), 0, 16)
#define LD64(off) __builtin_amdgcn_raw_buffer_load_b64(rs, (off), 0, 16)
#define ST128(v, off) __builtin_amdgcn_raw_buffer_store_b128((v), rs, (off), 0, 16)
#define ST64(v, off) __builtin_amdgcn_raw_buffer_store_b64((v), rs, (off), 0, 16)

#define STR_(x) #x
#define STR(x) STR_(x)
#define EX1(b) "v_exp_f32 v[" STR(b) "], v[" STR(b) "]\n\t"
#define EX4(b) EX1(b) EX1(b+1) EX1(b+2) EX1(b+3)
#define AR4(b) "a[" STR(b) ":" STR(b) "+3]"
#define KS(s) AR4(32+4*s)
#define V0(s) AR4(48+8*s)
#define V1(s) AR4(52+8*s)
#define SMF(acc, va, sb) "v_smfmac_f32_32x32x32_bf16 " acc ", " va ", v[" STR(sb) ":" STR(sb) "+7], %[idx]\n\t"
#define OA "a[0:15]"
#define OB "a[16:31]"
#define QKA(s) "v_mfma_f32_32x32x16_bf16 v[36:51], " KS(s) ", %[qa], v[68:83]\n\t"
#define QKB(s) "v_mfma_f32_32x32x16_bf16 v[52:67], " KS(s) ", %[qb], v[68:83]\n\t"
#define LDK(s, so) "buffer_load_dwordx4 " KS(s) ", %[vk], %[rs], " so " offen sc1\n\t"
#define LDV(s, so) "buffer_load_dwordx4 " V0(s) ", %[v0], %[rs], " so " offen sc1\n\t" \
                   "buffer_load_dwordx4 " V1(s) ", %[v1], %[rs], " so " offen sc1\n\t"
#define ACLOB "a0","a1","a2","a3","a4","a5","a6","a7","a8","a9","a10","a11","a12","a13","a14","a15","a16","a17","a18","a19", \
  "a20","a21","a22","a23","a24","a25","a26","a27","a28","a29","a30","a31","a32","a33","a34","a35","a36","a37","a38","a39", \
  "a40","a41","a42","a43","a44","a45","a46","a47","a48","a49","a50","a51","a52","a53","a54","a55","a56","a57","a58","a59", \
  "a60","a61","a62","a63","a64","a65","a66","a67","a68","a69","a70","a71","a72","a73","a74","a75","a76","a77","a78","a79"
#define TILE(s, sp, sn, soV, soK) \
  asm volatile(EX4(36) SMF(OB, V1(sp), 60) LDV(sp, "%[sv]") EX4(40) QKB(s) LDK(s, "%[sk]") EX4(44) \
               "s_waitcnt vmcnt(9)\n\t" SMF(OA, V0(s), 36) EX4(48) \
               EX4(52) SMF(OA, V1(s), 44) EX4(56) QKA(sn) EX4(60) SMF(OB, V0(s), 52) EX4(64) \
               : "+{v[36:51]}"(sa), "+{v[52:67]}"(sb) \
               : "{v[68:83]}"(mm), [qa] "v"(qa), [qb] "v"(qb), [idx] "v"(idx), [vk] "v"(vk), [v0] "v"(vv0), [v1] "v"(vv1), [rs] "s"(rs), \
                 [sv] "s"(soV), [sk] "s"(soK) : "memory", ACLOB)

__device__ __forceinline__ float max16(const f32x16& p) {
  float a = fmaxf(fmaxf(p[0], p[1]), p[2]), b = fmaxf(fmaxf(p[3], p[4]), p[5]);
  a = fmaxf(fmaxf(a, p[6]), p[7]); b = fmaxf(fmaxf(b, p[8]), p[9]);
  a = fmaxf(fmaxf(a, p[10]), p[11]); b = fmaxf(fmaxf(b, p[12]), p[13]);
  a = fmaxf(fmaxf(a, p[14]), p[15]);
  return fmaxf(a, b);
}
__device__ __forceinline__ float halves_max(float m) {
  auto rr = __builtin_amdgcn_permlane32_swap(__float_as_uint(m), __float_as_uint(m), false, false);
  return fmaxf(__uint_as_float(rr[0]), __uint_as_float(rr[1]));
}

__device__ __forceinline__ void TAIL(char* ws, const u16* x, u16* out, __amdgpu_buffer_rsrc_t rs, int b, int w,
    const float (&oa)[9], const float (&ob)[9], bf16x8 qa, bf16x8 qb, unsigned vk, unsigned vv0, unsigned vv1, unsigned idx,
    float gate, u32 tlo, u32 thi STARG) {
  constexpr unsigned l0 = 0;
  asm volatile("" : "+v"(vk));
  const int lane = vk >> 4;
  int hg, rb; MAPB(b, hg, rb);
  const int c31 = lane & 31, hh = lane >> 5, blk = (lane >> 4) & 1, q4 = (lane & 15) >> 2, p4 = lane & 3;
  const int tt = 2 * rb + (hg >> 2), cg = hg & 3, j = w & 3, kh = w >> 2, head = 4 * hg + j;
#pragma unroll
  for (int i = 0; i < 9; ++i) *(LAS float*)(size_t)(l0 + L_OS + (w ^ 4) * 4096 + i * 256 + lane * 4) = kh ? oa[i] : ob[i];
  unsigned best = 0;
  for (int spin = 0; spin < (SPIN_MAX << 4) && !best; ++spin) best = __builtin_amdgcn_readfirstlane(LDSU(L_CTL + 12));
  best &= 0x7fffffffu;
  BAR();
  float o[9];
  bool bad = false;
#pragma unroll
  for (int i = 0; i < 9; ++i) {
    o[i] = (kh ? ob[i] : oa[i]) + *(const LAS float*)(size_t)(l0 + L_OS + w * 4096 + i * 256 + lane * 4);
    bad |= !(fabsf(o[i]) < 1e30f);
  }
  bad |= !(o[8] > 1e-18f && o[8] < 1e18f);
  if (__any(bad)) {
    const bf16x8 q = kh ? qb : qa;
    const unsigned kb0 = WS_KV + head * 64 * KVB;
    float m = -3.0e38f;
#pragma unroll 1
    for (int t = 0; t < 64; ++t) {
      const f32x16 s = MFMA32(__builtin_bit_cast(bf16x8, LD128(kb0 + t * KVB + vk)), q, (f32x16{0}));
      m = fmaxf(m, max16(s));
    }
    m = -halves_max(m);
    f32x16 c, oc = {0};
#pragma unroll
    for (int i = 0; i < 16; ++i) c[i] = m;
#pragma unroll 1
    for (int t = 0; t < 64; ++t) {
      const f32x16 s = MFMA32(__builtin_bit_cast(bf16x8, LD128(kb0 + t * KVB + vk)), q, c);
      f32x8 e0, e1;
#pragma unroll
      for (int i = 0; i < 8; ++i) { e0[i] = __builtin_amdgcn_exp2f(s[i]); e1[i] = __builtin_amdgcn_exp2f(s[8 + i]); }
      const u32x4 va = __builtin_amdgcn_raw_buffer_load_b128(rs, vv0, kb0 + t * KVB, 16);
      const u32x4 vb = __builtin_amdgcn_raw_buffer_load_b128(rs, vv1, kb0 + t * KVB, 16);
      oc = __builtin_amdgcn_smfmac_f32_32x32x32_bf16(__builtin_bit_cast(bf16x8, va), __builtin_bit_cast(bf16x16, e0), oc, (int)idx, 0, 0);
      oc = __builtin_amdgcn_smfmac_f32_32x32x32_bf16(__builtin_bit_cast(bf16x8, vb), __builtin_bit_cast(bf16x16, e1), oc, (int)idx, 0, 0);
    }
#pragma unroll
    for (int i = 0; i < 9; ++i) o[i] = oc[i];
  }
  {
    const float rl = 1.f / o[8];
    const unsigned lim = 0xfffffffeu;
    ST128((u32x4{min(cvtpk(o[0] * rl, o[1] * rl), lim), min(cvtpk(o[2] * rl, o[3] * rl), lim), min(cvtpk(o[4] * rl, o[5] * rl), lim),
                 min(cvtpk(o[6] * rl, o[7] * rl), lim)}), WS_OF + ((2 * rb + kh) * 32 + head) * 1024 + vk);
  }
  ST(8);

  const int token = tt * 32 + c31, col = cg * 128 + (w & 3) * 32 + hh * 16;
  const int sel = 2047 - (int)(best & 2047u);
  u32x4 xa, xc;
  float yv = 0.f;
  const unsigned abase = (4 * hh + q4) * 256 + (((4 * (w & 3) + 2 * (p4 & 1) + blk) ^ (q4 << 2)) << 4) + 8 * (p4 >> 1);
  if (w < 4) {
    asm volatile(
      "ds_read_b64_tr_b16 a[0:1], %0 offset:0\n\t"
      "ds_read_b64_tr_b16 a[2:3], %0 offset:2048\n\t"
      "ds_read_b64_tr_b16 a[4:5], %0 offset:4096\n\t"
      "ds_read_b64_tr_b16 a[6:7], %0 offset:6144\n\t"
      "ds_read_b64_tr_b16 a[8:9], %0 offset:8192\n\t"
      "ds_read_b64_tr_b16 a[10:11], %0 offset:10240\n\t"
      "ds_read_b64_tr_b16 a[12:13], %0 offset:12288\n\t"
      "ds_read_b64_tr_b16 a[14:15], %0 offset:14336\n\t"
      "ds_read_b64_tr_b16 a[16:17], %0 offset:16384\n\t"
      "ds_read_b64_tr_b16 a[18:19], %0 offset:18432\n\t"
      "ds_read_b64_tr_b16 a[20:21], %0 offset:20480\n\t"
      "ds_read_b64_tr_b16 a[22:23], %0 offset:22528\n\t"
      "ds_read_b64_tr_b16 a[24:25], %0 offset:24576\n\t"
      "ds_read_b64_tr_b16 a[26:27], %0 offset:26624\n\t"
      "ds_read_b64_tr_b16 a[28:29], %0 offset:28672\n\t"
      "ds_read_b64_tr_b16 a[30:31], %0 offset:30720\n\t"
      "ds_read_b64_tr_b16 a[32:33], %0 offset:32768\n\t"
      "ds_read_b64_tr_b16 a[34:35], %0 offset:34816\n\t"
      "ds_read_b64_tr_b16 a[36:37], %0 offset:36864\n\t"
      "ds_read_b64_tr_b16 a[38:39], %0 offset:38912\n\t"
      "ds_read_b64_tr_b16 a[40:41], %0 offset:40960\n\t"
      "ds_read_b64_tr_b16 a[42:43], %0 offset:43008\n\t"
      "ds_read_b64_tr_b16 a[44:45], %0 offset:45056\n\t"
      "ds_read_b64_tr_b16 a[46:47], %0 offset:47104\n\t"
      "ds_read_b64_tr_b16 a[48:49], %0 offset:49152\n\t"
      "ds_read_b64_tr_b16 a[50:51], %0 offset:51200\n\t"
      "ds_read_b64_tr_b16 a[52:53], %0 offset:53248\n\t"
      "ds_read_b64_tr_b16 a[54:55], %0 offset:55296\n\t"
      "ds_read_b64_tr_b16 a[56:57], %0 offset:57344\n\t"
      "ds_read_b64_tr_b16 a[58:59], %0 offset:59392\n\t"
      "ds_read_b64_tr_b16 a[60:61], %0 offset:61440\n\t"
      "ds_read_b64_tr_b16 a[62:63], %0 offset:63488\n\t"
      "ds_read_b64_tr_b16 a[64:65], %1 offset:0\n\t"
      "ds_read_b64_tr_b16 a[66:67], %1 offset:2048\n\t"
      "ds_read_b64_tr_b16 a[68:69], %1 offset:4096\n\t"
      "ds_read_b64_tr_b16 a[70:71], %1 offset:6144\n\t"
      "ds_read_b64_tr_b16 a[72:73], %1 offset:8192\n\t"
      "ds_read_b64_tr_b16 a[74:75], %1 offset:10240\n\t"
      "ds_read_b64_tr_b16 a[76:77], %1 offset:12288\n\t"
      "ds_read_b64_tr_b16 a[78:79], %1 offset:14336\n\t"
      :: "v"(abase), "v"(abase + 65536) : "memory", ACLOB);
    xa = *(const u32x4*)(x + (size_t)token * D_ + col); xc = *(const u32x4*)(x + (size_t)token * D_ + col + 8);
    if ((sel >> 5) == tt) {
      for (int spin = 0; spin < SPIN_MAX; ++spin) {
      if (spin) __builtin_amdgcn_s_sleep(4);
      FRESH();
        bool ok = true; yv = 0.f;
#pragma unroll
        for (int i = 0; i < 4; ++i) {
          const u32x4 a = LD128(WS_Y + ((4 * hh + i) * 512 + cg * 128 + w * 32 + c31) * 16);
          ok &= a[1] == thi && a[3] == thi && ((a[0] ^ tlo) | (a[2] ^ tlo)) < 0x10000u;
          yv += __uint_as_float((a[0] & 0xffffu) | (a[2] << 16));
        }
        if (__all(ok)) break;
      }
      yv += __shfl_xor(yv, 32);
    }
  }
  float tb[16];
  if (w < 4) {
#pragma unroll
    for (int i = 0; i < 4; ++i) { tb[2 * i] = bflo(xa[i]); tb[2 * i + 1] = bfhi(xa[i]); tb[8 + 2 * i] = bflo(xc[i]); tb[9 + 2 * i] = bfhi(xc[i]); }
    if ((sel >> 5) == tt) {
      const unsigned srt = best >> 11;
      const float rsel = bfround(sigmoidf_(bflo((srt & 0x8000u) ? (srt & 0x7fffu) : (~srt & 0xffffu))));
      const float yy = bfround(bfround(yv) * rsel);
#pragma unroll
      for (int i = 0; i < 16; ++i) { const float t = __shfl(yy, hh * 16 + i); if (token == sel) tb[i] = bfround(tb[i] + t); }
    }
    const float og = bfround(1.f - gate);
#pragma unroll
    for (int i = 0; i < 16; ++i) tb[i] *= og;
  }
  {
    u32x4 v[4];
    for (int spin = 0; spin < SPIN_MAX; ++spin) {
      if (spin) __builtin_amdgcn_s_sleep(4);
      FRESH();
      unsigned m = 0;
#pragma unroll
      for (int i = 0; i < 4; ++i) v[i] = LD128(WS_OF + (tt * 32 + 4 * w + i) * 1024 + vk);
#pragma unroll
      for (int i = 0; i < 4; ++i) m = max(max(m, max(v[i][0], v[i][1])), max(v[i][2], v[i][3]));
      if (!__any(m == ~0u)) break;
    }
    ST(9);
#pragma unroll
    for (int i = 0; i < 4; ++i) lds_w128(l0 + L_OS + (4 * w + i) * 1024 + vk, v[i]);
  }
  BAR();
  ST(10);
  if (w < 4) {
    f32x16 a;
    asm volatile(
      "ds_read_b128 v[20:23], %[b] offset:0\n\t"
      "ds_read_b128 v[24:27], %[b] offset:1024\n\t"
      "ds_read_b128 v[28:31], %[b] offset:2048\n\t"
      "ds_read_b128 v[32:35], %[b] offset:3072\n\t"
      "ds_read_b128 v[36:39], %[b] offset:4096\n\t"
      "ds_read_b128 v[40:43], %[b] offset:5120\n\t"
      "ds_read_b128 v[44:47], %[b] offset:6144\n\t"
      "ds_read_b128 v[48:51], %[b] offset:7168\n\t"
      "s_waitcnt lgkmcnt(7)\n\t"
      "v_mfma_f32_32x32x16_bf16 v[68:83], a[0:3], v[20:23], 0\n\t"
      "ds_read_b128 v[20:23], %[b] offset:8192\n\t"
      "ds_read_b64_tr_b16 a[0:1], %[a1] offset:16384\n\t"
      "ds_read_b64_tr_b16 a[2:3], %[a1] offset:18432\n\t"
      "s_waitcnt lgkmcnt(9)\n\t"
      "v_mfma_f32_32x32x16_bf16 v[68:83], a[4:7], v[24:27], v[68:83]\n\t"
      "ds_read_b128 v[24:27], %[b] offset:9216\n\t"
      "ds_read_b64_tr_b16 a[4:5], %[a1] offset:20480\n\t"
      "ds_read_b64_tr_b16 a[6:7], %[a1] offset:22528\n\t"
      "s_waitcnt lgkmcnt(11)\n\t"
      "v_mfma_f32_32x32x16_bf16 v[68:83], a[8:11], v[28:31], v[68:83]\n\t"
      "ds_read_b128 v[28:31], %[b] offset:10240\n\t"
      "ds_read_b64_tr_b16 a[8:9], %[a1] offset:24576\n\t"
      "ds_read_b64_tr_b16 a[10:11], %[a1] offset:26624\n\t"
      "s_waitcnt lgkmcnt(13)\n\t"
      "v_mfma_f32_32x32x16_bf16 v[68:83], a[12:15], v[32:35], v[68:83]\n\t"
      "ds_read_b128 v[32:35], %[b] offset:11264\n\t"
      "ds_read_b64_tr_b16 a[12:13], %[a1] offset:28672\n\t"
      "ds_read_b64_tr_b16 a[14:15], %[a1] offset:30720\n\t"
      "s_waitcnt lgkmcnt(15)\n\t"
      "v_mfma_f32_32x32x16_bf16 v[68:83], a[16:19], v[36:39], v[68:83]\n\t"
      "ds_read_b128 v[36:39], %[b] offset:12288\n\t"
      "ds_read_b64_tr_b16 a[16:17], %[a1] offset:32768\n\t"
      "ds_read_b64_tr_b16 a[18:19], %[a1] offset:34816\n\t"
      "s_waitcnt lgkmcnt(15)\n\t"
      "v_mfma_f32_32x32x16_bf16 v[68:83], a[20:23], v[40:43], v[68:83]\n\t"
      "ds_read_b128 v[40:43], %[b] offset:13312\n\t"
      "ds_read_b64_tr_b16 a[20:21], %[a1] offset:36864\n\t"
      "ds_read_b64_tr_b16 a[22:23], %[a1] offset:38912\n\t"
      "s_waitcnt lgkmcnt(15)\n\t"
      "v_mfma_f32_32x32x16_bf16 v[68:83], a[24:27], v[44:47], v[68:83]\n\t"
      "ds_read_b128 v[44:47], %[b] offset:14336\n\t"
      "ds_read_b64_tr_b16 a[24:25], %[a1] offset:40960\n\t"
      "ds_read_b64_tr_b16 a[26:27], %[a1] offset:43008\n\t"
      "s_waitcnt lgkmcnt(15)\n\t"
      "v_mfma_f32_32x32x16_bf16 v[68:83], a[28:31], v[48:51], v[68:83]\n\t"
      "ds_read_b128 v[48:51], %[b] offset:15360\n\t"
      "ds_read_b64_tr_b16 a[28:29], %[a1] offset:45056\n\t"
      "ds_read_b64_tr_b16 a[30:31], %[a1] offset:47104\n\t"
      "s_waitcnt lgkmcnt(15)\n\t"
      "v_mfma_f32_32x32x16_bf16 v[68:83], a[32:35], v[20:23], v[68:83]\n\t"
      "ds_read_b128 v[20:23], %[b] offset:16384\n\t"
      "ds_read_b64_tr_b16 a[32:33], %[a1] offset:49152\n\t"
      "ds_read_b64_tr_b16 a[34:35], %[a1] offset:51200\n\t"
      "s_waitcnt lgkmcnt(15)\n\t"
      "v_mfma_f32_32x32x16_bf16 v[68:83], a[36:39], v[24:27], v[68:83]\n\t"
      "ds_read_b128 v[24:27], %[b] offset:17408\n\t"
      "ds_read_b64_tr_b16 a[36:37], %[a1] offset:53248\n\t"
      "ds_read_b64_tr_b16 a[38:39], %[a1] offset:55296\n\t"
      "s_waitcnt lgkmcnt(15)\n\t"
      "v_mfma_f32_32x32x16_bf16 v[68:83], a[40:43], v[28:31], v[68:83]\n\t"
      "ds_read_b128 v[28:31], %[b] offset:18432\n\t"
      "ds_read_b64_tr_b16 a[40:41], %[a1] offset:57344\n\t"
      "ds_read_b64_tr_b16 a[42:43], %[a1] offset:59392\n\t"
      "s_waitcnt lgkmcnt(15)\n\t"
      "v_mfma_f32_32x32x16_bf16 v[68:83], a[44:47], v[32:35], v[68:83]\n\t"
      "ds_read_b128 v[32:35], %[b] offset:19456\n\t"
      "ds_read_b64_tr_b16 a[44:45], %[a1] offset:61440\n\t"
      "ds_read_b64_tr_b16 a[46:47], %[a1] offset:63488\n\t"
      "s_waitcnt lgkmcnt(15)\n\t"
      "v_mfma_f32_32x32x16_bf16 v[68:83], a[48:51], v[36:39], v[68:83]\n\t"
      "ds_read_b128 v[36:39], %[b] offset:20480\n\t"
      "s_waitcnt lgkmcnt(15)\n\t"
      "v_mfma_f32_32x32x16_bf16 v[68:83], a[52:55], v[40:43], v[68:83]\n\t"
      "ds_read_b128 v[40:43], %[b] offset:21504\n\t"
      "s_waitcnt lgkmcnt(15)\n\t"
      "v_mfma_f32_32x32x16_bf16 v[68:83], a[56:59], v[44:47], v[68:83]\n\t"
      "ds_read_b128 v[44:47], %[b] offset:22528\n\t"
      "s_waitcnt lgkmcnt(15)\n\t"
      "v_mfma_f32_32x32x16_bf16 v[68:83], a[60:63], v[48:51], v[68:83]\n\t"
      "ds_read_b128 v[48:51], %[b] offset:23552\n\t"
      "s_waitcnt lgkmcnt(15)\n\t"
      "v_mfma_f32_32x32x16_bf16 v[68:83], a[64:67], v[20:23], v[68:83]\n\t"
      "ds_read_b128 v[20:23], %[b] offset:24576\n\t"
      "s_waitcnt lgkmcnt(13)\n\t"
      "v_mfma_f32_32x32x16_bf16 v[68:83], a[68:71], v[24:27], v[68:83]\n\t"
      "ds_read_b128 v[24:27], %[b] offset:25600\n\t"
      "s_waitcnt lgkmcnt(11)\n\t"
      "v_mfma_f32_32x32x16_bf16 v[68:83], a[72:75], v[28:31], v[68:83]\n\t"
      "ds_read_b128 v[28:31], %[b] offset:26624\n\t"
      "s_waitcnt lgkmcnt(9)\n\t"
      "v_mfma_f32_32x32x16_bf16 v[68:83], a[76:79], v[32:35], v[68:83]\n\t"
      "ds_read_b128 v[32:35], %[b] offset:27648\n\t"
      "s_waitcnt lgkmcnt(7)\n\t"
      "v_mfma_f32_32x32x16_bf16 v[68:83], a[0:3], v[36:39], v[68:83]\n\t"
      "ds_read_b128 v[36:39], %[b] offset:28672\n\t"
      "s_waitcnt lgkmcnt(7)\n\t"
      "v_mfma_f32_32x32x16_bf16 v[68:83], a[4:7], v[40:43], v[68:83]\n\t"
      "ds_read_b128 v[40:43], %[b] offset:29696\n\t"
      "s_waitcnt lgkmcnt(7)\n\t"
      "v_mfma_f32_32x32x16_bf16 v[68:83], a[8:11], v[44:47], v[68:83]\n\t"
      "ds_read_b128 v[44:47], %[b] offset:30720\n\t"
      "s_waitcnt lgkmcnt(7)\n\t"
      "v_mfma_f32_32x32x16_bf16 v[68:83], a[12:15], v[48:51], v[68:83]\n\t"
      "ds_read_b128 v[48:51], %[b] offset:31744\n\t"
      "s_waitcnt lgkmcnt(7)\n\t"
      "v_mfma_f32_32x32x16_bf16 v[68:83], a[16:19], v[20:23], v[68:83]\n\t"
      "s_waitcnt lgkmcnt(6)\n\t"
      "v_mfma_f32_32x32x16_bf16 v[68:83], a[20:23], v[24:27], v[68:83]\n\t"
      "s_waitcnt lgkmcnt(5)\n\t"
      "v_mfma_f32_32x32x16_bf16 v[68:83], a[24:27], v[28:31], v[68:83]\n\t"
      "s_waitcnt lgkmcnt(4)\n\t"
      "v_mfma_f32_32x32x16_bf16 v[68:83], a[28:31], v[32:35], v[68:83]\n\t"
      "s_waitcnt lgkmcnt(3)\n\t"
      "v_mfma_f32_32x32x16_bf16 v[68:83], a[32:35], v[36:39], v[68:83]\n\t"
      "s_waitcnt lgkmcnt(2)\n\t"
      "v_mfma_f32_32x32x16_bf16 v[68:83], a[36:39], v[40:43], v[68:83]\n\t"
      "s_waitcnt lgkmcnt(1)\n\t"
      "v_mfma_f32_32x32x16_bf16 v[68:83], a[40:43], v[44:47], v[68:83]\n\t"
      "s_waitcnt lgkmcnt(0)\n\t"
      "v_mfma_f32_32x32x16_bf16 v[68:83], a[44:47], v[48:51], v[68:83]\n\t"
      "s_nop 15\n\t"
      "s_nop 3\n\t"
      : "=&{v[68:83]}"(a) : [b] "v"(L_OS + vk), [a1] "v"(abase + 65536) : "memory", ACLOB, "v20","v21","v22","v23","v24","v25","v26","v27","v28","v29","v30","v31","v32","v33","v34","v35","v36","v37","v38","v39","v40","v41","v42","v43","v44","v45","v46","v47","v48","v49","v50","v51");
    float r[16];
#pragma unroll
    for (int i = 0; i < 16; ++i) r[i] = fmaf(gate, a[i], tb[i]);
    const rsrc_t ro_ = mkrs(out, 2048 * D_ * 2);
    __builtin_amdgcn_raw_buffer_store_b128((u32x4{cvtpk(r[0], r[1]), cvtpk(r[2], r[3]), cvtpk(r[4], r[5]), cvtpk(r[6], r[7])}), ro_, (token * D_ + col) * 2, 0, 16);
    __builtin_amdgcn_raw_buffer_store_b128((u32x4{cvtpk(r[8], r[9]), cvtpk(r[10], r[11]), cvtpk(r[12], r[13]), cvtpk(r[14], r[15])}), ro_, (token * D_ + col) * 2 + 16, 0, 16);
    if (b == 0 && w == 0 && lane == 0) ST64((u32x2{tlo, thi}), WS_EP);
  }
  ST(11);
}

__global__ void __launch_bounds__(576, 3)
fused(const u16* __restrict__ x, const u16* __restrict__ W, const u16* __restrict__ wo, const u16* __restrict__ wr,
      const u16* __restrict__ wg, const u16* __restrict__ wu, const u16* __restrict__ wd, const u16* __restrict__ gw,
      char* __restrict__ ws, u16* __restrict__ out) {
  Stamps stp; asm volatile("s_memrealtime %0\n\ts_waitcnt lgkmcnt(0)" : "=s"(stp.t0) :: "memory"); stp.t = stp.t0;
  constexpr unsigned l0 = 0;
  const int b = blockIdx.x;
  int hg, rb; MAPB(b, hg, rb);
  const int lane = threadIdx.x & 63, w = __builtin_amdgcn_readfirstlane(threadIdx.x >> 6);
  const int c31 = lane & 31, hh = lane >> 5, blk = (lane >> 4) & 1, q4 = (lane & 15) >> 2, p4 = lane & 3;
  const int tt = 2 * rb + (hg >> 2), cg = hg & 3;
  const __amdgpu_buffer_rsrc_t rs = __builtin_amdgcn_make_buffer_rsrc(ws, 0, (int)WS_END, 0x00020000);
  STDECL
  ST(0);
  asm volatile("" :: "s"(x), "s"(W), "s"(wr), "s"(gw), "s"(ws));
#define GET_TAG() u64 ep_; asm volatile("s_load_dwordx2 %0, %1, 0x0\n\ts_waitcnt lgkmcnt(0)" : "=s"(ep_) : "s"(ws + WS_EP) : "memory"); \
  const u64 tag = ep_ * 0x9E3779B97F4A7C15ull + 0xD1B54A32D192ED03ull; const u32 thi = (u32)(tag >> 32), tlo = (u32)tag

  if (w == 8) {
#pragma unroll
    for (int i = 0; i < 8; ++i) ST128((u32x4{~0u, ~0u, ~0u, ~0u}), WS_OF + ((2 * rb + (i >> 2)) * 32 + 4 * hg + (i & 3)) * 1024 + lane * 16);
    if (lane < 4) LDSU(L_CTL + lane * 4) = 0;
    GET_TAG();
#pragma unroll 1
    for (int i = 0; i < 6; ++i) BAR();
    DRAIN();
    if (lane == 0) ST64((u32x2{tlo, thi}), WS_OL + (rb * 8 + hg) * 128);
#pragma unroll 1
    for (int i = 0; i < 5; ++i) BAR();
    const int cg1 = (b & 7) * 28 + (b >> 3);
    const int ks2 = b & 7, cg2 = b >> 3;
    const bool has1 = (b >> 3) < 28;
    ST(2);
    __builtin_amdgcn_s_setprio(HPRIO);
    const rsrc_t ro = mkrs(wo, D_ * D_ * 2);
    const unsigned vwo = (lane >> 4) * (D_ * 2) + cg * 256 + (((lane & 15) ^ ((lane >> 4) << 2)) * 16);
    unsigned key = 0;
    for (int spin = 0; spin < SPIN_MAX; ++spin) {
      if (spin) __builtin_amdgcn_s_sleep(2);
      FRESH();
      const u32x2 f = LD64(WS_FL + (hg * 32 + ((lane + rb) & 31)) * 128);
      key = f[0] & 0x7ffffffu;
      const unsigned rdy = (unsigned)__ballot(f[1] == thi && (f[0] ^ tlo) < 0x8000000u);
      if (lane == 0) LDSU(L_CTL) = rdy;
      if (rdy == ~0u) break;
    }
    if (lane == 0) LDSU(L_CTL) = ~0u;
    ST(3);
    const unsigned best = wave_max_u32(key);
    const int sel = 2047 - (int)(best & 2047u);
    if (lane == 0) LDSU(L_CTL + 12) = best | 0x80000000u;
    if (has1) {
      const rsrc_t rg = mkrs(wg, D_ * F_ * 2), ru = mkrs(wu, D_ * F_ * 2);
      const unsigned vo = (lane >> 1) * (F_ * 2) + cg1 * 16;
#pragma unroll 1
      for (int i = 0; i < 16; ++i)
        asm volatile("s_mov_b32 m0, %0\n\ts_mov_b64 exec, %5\n\tbuffer_load_dwordx4 %1, %2, %4 offen lds\n\ts_not_b64 exec, exec\n\t"
                     "buffer_load_dwordx4 %1, %3, %4 offen lds\n\ts_mov_b64 exec, -1"
                     :: "s"(L_GU + i * 1024), "v"(vo), "s"(rg), "s"(ru), "s"(i * 32 * F_ * 2), "s"(0x5555555555555555ull) : "memory", "scc");
    }
    {
      const rsrc_t rd = mkrs(wd, D_ * F_ * 2);
      const unsigned vo = (ks2 * 224 + (lane >> 1)) * (D_ * 2) + cg2 * 32 + (lane & 1) * 16;
#pragma unroll 1
      for (int i = 0; i < 7; ++i) dma<L_WD>(vo, rd, i * 32 * D_ * 2, i * 1024);
    }
    if (has1) {
      dma<L_XS>(lane * 16, mkrs(x, 2048 * D_ * 2), sel * (D_ * 2), 0);
      ST(4);
      DRAIN();
      ST(5);
      f32x4 d = {0};
      const unsigned ba = l0 + L_GU + (8 * (lane >> 4) + q4) * 32 + p4 * 8;
#pragma unroll 4
      for (int u = 0; u < 16; ++u)
        d = MFMA16(lds_b128(l0 + L_XS + (4 * u + (lane >> 4)) * 16), cat8(lds_tr(ba + u * 1024), lds_tr(ba + u * 1024 + 128)), d);
      const float gp = bfround(d[0]), up = bfround(__shfl_down(d[0], 8));
      const unsigned hb = cvtpk(bfround(gp * sigmoidf_(gp)) * up, 0.f) & 0xffffu;
      if (lane < 8) ST64((u32x2{(tlo & 0xffff0000u) | hb, thi}), WS_H + (cg1 * 8 + lane) * 8);
    }
    ST(6);
#pragma unroll 1
    for (int i = 25; i < 80; ++i) dma<0>(vwo, ro, i * 4096, i * 1024);
    ST(7);
    for (int spin = 0; spin < SPIN_MAX; ++spin) {
      if (spin) __builtin_amdgcn_s_sleep(4);
      FRESH();
      const unsigned o = WS_H + ks2 * 1792 + (lane < 56 ? lane : 0) * 32;
      const u32x4 a = LD128(o), c = LD128(o + 16);
      const bool ok = a[1] == thi && a[3] == thi && c[1] == thi && c[3] == thi &&
                      ((a[0] ^ tlo) | (a[2] ^ tlo) | (c[0] ^ tlo) | (c[2] ^ tlo)) < 0x10000u;
      *(LAS u32x2*)(size_t)(l0 + L_HS + lane * 8) = u32x2{(a[0] & 0xffffu) | (a[2] << 16), (c[0] & 0xffffu) | (c[2] << 16)};
      if (__all(ok)) break;
    }
    ST(8);
    {
      f32x4 d = {0};
      const unsigned ba = l0 + L_WD + (8 * (lane >> 4) + q4) * 32 + p4 * 8;
#pragma unroll
      for (int u = 0; u < 7; ++u)
        d = MFMA16(lds_b128(l0 + L_HS + (4 * u + (lane >> 4)) * 16), cat8(lds_tr(ba + u * 1024), lds_tr(ba + u * 1024 + 128)), d);
      const unsigned yb = __float_as_uint(d[0]);
      if (lane < 16) ST128((u32x4{(tlo & 0xffff0000u) | (yb & 0xffffu), thi, (tlo & 0xffff0000u) | (yb >> 16), thi}),
                           WS_Y + (ks2 * 512 + cg2 * 16 + lane) * 16);
      asm volatile("s_waitcnt lgkmcnt(0)" ::: "memory");
    }
    ST(9);
#pragma unroll 1
    for (int i = 80; i < 128 + 25; ++i) { const int r = i & 127; dma<0>(vwo, ro, r * 4096, r * 1024); }
    for (int spin = 0; spin < SPIN_MAX; ++spin) {
      if (spin) __builtin_amdgcn_s_sleep(4);
      FRESH();
      const u32x2 f = LD64(WS_OL + (rb * 8 + (lane & 7)) * 128);
      if (__all(f[0] == tlo && f[1] == thi)) break;
    }
    ST(1);
    WAIT_BAR(0);
    BAR();
    STDUMP();
    return;
  }

  const int rot = (rb + hg) & 7;
  const rsrc_t rx = mkrs(x, 2048 * D_ * 2), rw = mkrs(W, D_ * 1536 * 2);
  const unsigned mw = w * 1024;
  const unsigned vx = (rb * 64 + w * 8 + (lane >> 3)) * 1024 + (((lane & 7) ^ ((w * 4 + (lane >> 4)) & 7)) << 4);
  const unsigned vq = ((lane >> 3) + w * 8) * 3072 + hg * 128 + (((lane & 7) ^ (((lane >> 4) & 1) << 2)) << 4);
  const unsigned vkv = vq + (w < 4 ? 1024 : 2048 - 32 * 3072);
  if (w == 4) dma<L_WV>(((lane + 8 * rot) & 63) * 16, mkrs(wr, 1024), 0, 0);
  if (w == 5) dma<L_WV + 1024>(((lane + 8 * rot) & 63) * 16, mkrs(gw, 1024), 0, 0);
#define KBLK(g) ((unsigned)(((g) + rot) & 7))
#define ISSX(g) dma<L_X + (g) * 8192>(vx, rx, KBLK(g) * 128, mw)
#define ISSUE(r) do { if ((r) < 16) dma<L_W + (((r) + 4) % 10) * 8192>(vkv, rw, KBLK((r) >> 1) * 196608 + ((r) & 1) * 98304, mw); \
                      else dma<L_W + (((r) + 4) % 10) * 8192>(vq, rw, KBLK(r) * 196608, mw); } while (0)
#define ISSG(g) do { ISSX(g); ISSUE(2 * (g)); ISSUE(2 * (g) + 1); } while (0)
#define LEAD 4
  ISSG(0); ISSG(1); ISSG(2);
  if (LEAD == 4) ISSG(3);

  const int part = w >> 2, pair = w & 1, tile = (w >> 1) & 1;
  unsigned xa0 = L_X + (tile * 32 + c31) * 128 + ((hh ^ ((c31 >> 1) & 7)) << 4);
  unsigned xa1 = xa0 ^ 32, xa2 = xa0 ^ 64, xa3 = xa0 ^ 96;
  unsigned wq0 = L_W + (8 * hh + q4) * 128 + (((4 * pair + 2 * blk + (p4 >> 1)) ^ (((q4 >> 1) & 1) << 2)) << 4) + 8 * (p4 & 1);
  unsigned wa0 = wq0 + part * 4096, wa1 = wa0 + 65536;
  asm volatile("" : "+v"(xa0), "+v"(xa1), "+v"(xa2), "+v"(xa3), "+v"(wa0), "+v"(wa1), "+v"(wq0));
  f32x16 acc = {0};
  GET_TAG();
  {
    bf16x8 wf[2][4], xf[2][4];
#define WADDR(g, u) ((((2 * (g) + 4 + ((u) >> 1)) % 10) >= 8 ? wa1 - 65536 : wa0) + ((2 * (g) + 4 + ((u) >> 1)) % 10) * 8192 + ((u) & 1) * 2048)
#define RD1(g, u, xa) do { xf[(g) & 1][u] = lds_b128(xa + (g) * 8192); \
      wf[(g) & 1][u] = cat8(lds_tr(WADDR(g, u)), lds_tr(WADDR(g, u) + 512)); } while (0)
#define RD4(g) do { RD1(g, 0, xa0); RD1(g, 1, xa1); RD1(g, 2, xa2); RD1(g, 3, xa3); } while (0)
#define MM(V, g, u) do { if (V) acc = MFMA32(xf[(g) & 1][u], wf[(g) & 1][u], acc); else acc = MFMA32(wf[(g) & 1][u], xf[(g) & 1][u], acc); SBAR(); } while (0)
#define NXT(g, i) do { if ((g) + LEAD <= 7) { if ((i) == 0) ISSX(((g) + LEAD) & 7); else ISSUE(2 * ((g) + LEAD) + (i) - 1); } \
      if ((g) >= 4 && (i) > 0) ISSUE(2 * (g) + 7 + (i)); } while (0)
#define GROUP(V, g, N) do { WAIT_BAR(N); STG(4 + (g)); MM(V, (g) - 1, 0); RD1(g, 0, xa0); NXT(g, 0); SBAR(); MM(V, (g) - 1, 1); RD1(g, 1, xa1); NXT(g, 1); SBAR(); \
      MM(V, (g) - 1, 2); RD1(g, 2, xa2); NXT(g, 2); SBAR(); MM(V, (g) - 1, 3); RD1(g, 3, xa3); SBAR(); } while (0)
#define WB0 9
#define KVLOOP_(V, N) do { WAIT_BAR(N); ST(1); RD4(0); NXT(0, 0); NXT(0, 1); NXT(0, 2); SBAR(); GROUP(V, 1, N); GROUP(V, 2, N); GROUP(V, 3, N); GROUP(V, 4, N); \
      GROUP(V, 5, 8); GROUP(V, 6, 7); if (LEAD == 4) GROUP(V, 7, 6); else GROUP(V, 7, 5); MM(V, 7, 0); MM(V, 7, 1); MM(V, 7, 2); MM(V, 7, 3); } while (0)
#define KVLOOP__(V, N) KVLOOP_(V, N)
#define KVLOOP(V) KVLOOP__(V, WB0)
    if (part == 0) KVLOOP(false); else KVLOOP(true);
  }
  ST(2);
  const u32x4 p0 = pack8(acc, 0), p1 = pack8(acc, 1);
  unsigned o0, o1;
  int h0, h1;
  if (part == 0) {
    const int s23 = (c31 & 19) | ((c31 & 4) << 1) | ((c31 & 8) >> 1);
    o0 = o1 = (s23 + 32 * hh) * 16; h0 = 2 * pair; h1 = 2 * pair + 1;
  } else {
    o0 = 1024 + (hh * 16 + (lane & 15)) * 16; o1 = o0 + 512; h0 = h1 = 2 * pair + blk;
  }
  ST128(p0, WS_KV + (((4 * hg + h0) * 2 + tile) * 32 + rb) * KVB + o0);
  ST128(p1, WS_KV + (((4 * hg + h1) * 2 + tile) * 32 + rb) * KVB + o1);
  if (part == 1 && lane < 2) ST128((u32x4{0x3F803F80u, 0x3F803F80u, 0x3F803F80u, 0x3F803F80u}), WS_KV + (((4 * hg + 2 * pair + lane) * 2 + tile) * 32 + rb) * KVB + 2048);
  if (part == 0) WAIT_BAR(2); else WAIT_BAR(3);
  ST(13);
  lds_w128(l0 + L_KV + (h0 * 2 + tile) * 2048 + o0, p0);
  lds_w128(l0 + L_KV + (h1 * 2 + tile) * 2048 + o1, p1);
  if (part == 0) {
    f32x16 qacc = {0};
    bf16x8 wf[2][4], xf[2][4];
#define QR1(r, u, xa) do { xf[(r) & 1][u] = lds_b128(xa + (r) * 8192); \
      wf[(r) & 1][u] = cat8(lds_tr(wq0 + (r) * 8192 + (u) * 2048), lds_tr(wq0 + (r) * 8192 + (u) * 2048 + 512)); } while (0)
#define QR4(r) do { QR1(r, 0, xa0); QR1(r, 1, xa1); QR1(r, 2, xa2); QR1(r, 3, xa3); } while (0)
    QR4(0);
    SBAR();
#define QS(r, u, xa) do { qacc = MFMA32(wf[(r) & 1][u], xf[(r) & 1][u], qacc); SBAR(); if ((r) < 7) QR1((r) + 1, u, xa); SBAR(); } while (0)
#define QG(r) do { QS(r, 0, xa0); QS(r, 1, xa1); QS(r, 2, xa2); QS(r, 3, xa3); } while (0)
    QG(0); QG(1); QG(2); QG(3); QG(4); QG(5); QG(6); QG(7);
    const float qs = 0.25f * 1.4426950408889634f;
    lds_w128(l0 + L_QX + ((2 * pair) * 2 + tile) * 1024 + lane * 16, pack8(qacc, 0, qs));
    lds_w128(l0 + L_QX + ((2 * pair + 1) * 2 + tile) * 1024 + lane * 16, pack8(qacc, 1, qs));
  } else {
    const int tok = 16 * (w & 3) + (lane & 15), g4 = lane >> 4;
    f32x4 d = {0};
    unsigned fa0 = L_WV + (lane & 1) * 1024 + g4 * 16, fb0 = L_X + tok * 128 + ((g4 ^ ((tok >> 1) & 7)) << 4), fb1 = fb0 ^ 64;
    asm volatile("" : "+v"(fa0), "+v"(fb0), "+v"(fb1));
    bf16x8 fa[16], fb[16];
#define RGR(u) do { fa[u] = lds_b128(fa0 + (u) * 64); fb[u] = lds_b128((((u) & 1) ? fb1 : fb0) + ((u) >> 1) * 8192); } while (0)
    RGR(0); RGR(1); RGR(2); RGR(3);
    SBAR();
#pragma unroll
    for (int u = 0; u < 16; ++u) { d = MFMA16(fa[u], fb[u], d); SBAR(); if (u + 4 < 16) RGR((u + 4) & 15); SBAR(); }
    const unsigned r16 = cvtpk(d[0], 0.f) & 0xffffu;
    unsigned key = (((r16 & 0x8000u) ? (~r16 & 0xffffu) : (r16 | 0x8000u)) << 11) | (2047 - (rb * 64 + tok));
    if (lane < 16) {
      asm volatile("ds_max_u32 %0, %1" :: "v"(L_CTL + 8), "v"(key) : "memory");
      *(LAS float*)(size_t)(l0 + L_RG + tok * 4) = bfround(sigmoidf_(bfround(d[1])));
    }
    if (w == 7 && lane < 2) { const unsigned v = lane ? 0u : 0x3F803F80u; lds_w128(l0 + L_ONE + lane * 16, u32x4{v, v, v, v}); }
  }
  ST(3);
  BAR();
  DRAIN();
  if (lane == 0 && __hip_atomic_fetch_add((LAS u32*)(size_t)(L_CTL + 4), 1u, __ATOMIC_RELAXED, __HIP_MEMORY_SCOPE_WORKGROUP) == 7)
    ST64((u32x2{(tlo & 0xf8000000u) | LDSU(L_CTL + 8), thi}), WS_FL + (hg * 32 + rb) * 128);
  ST(14);

  const int j = w & 3, kh = w >> 2, head = 4 * hg + j;
  const bool onerow = c31 == 16 || c31 == 20;
  const unsigned idx = 0xDDDDDDDDu, vk = lane * 16;
  const unsigned vv0 = c31 < 16 ? 1024 + (hh * 16 + c31) * 16 : (onerow ? 2048u : 0x7f000000u);
  const unsigned vv1 = c31 < 16 ? vv0 + 512 : vv0;
  const bf16x8 qa = lds_b128(l0 + L_QX + (j * 2) * 1024 + lane * 16), qb = lds_b128(l0 + L_QX + (j * 2 + 1) * 1024 + lane * 16);
  const float gate = *(const LAS float*)(size_t)(l0 + L_RG + ((hg >> 2) * 32 + c31) * 4);
  f32x16 sa, sb, mm;
  {
    const unsigned lk = l0 + L_KV + (j * 2 + kh) * 2048;
    const unsigned la = lk + vk, lm = l0 + L_KV + j * 4096 + vk;
    const unsigned lv0 = c31 < 16 ? lk + vv0 : l0 + L_ONE + (onerow ? 0 : 16), lv1 = c31 < 16 ? lv0 + 512 : lv0;
#define Z1(r) "v_accvgpr_write_b32 a[" STR(r) "], 0\n\t"
#define Z4(r) Z1(r) Z1(r+1) Z1(r+2) Z1(r+3)
#define Z16(r) Z4(r) Z4(r+4) Z4(r+8) Z4(r+12)
    asm volatile("ds_read_b128 " KS(1) ", %[lm]\n\tds_read_b128 " KS(0) ", %[la]\n\tds_read_b128 " V0(0) ", %[a0]\n\tds_read_b128 " V1(0) ", %[a1]\n\t"
                 Z16(0) Z16(16)
                 "s_waitcnt lgkmcnt(0)\n\ts_barrier\n\t"
                 "v_mfma_f32_32x32x16_bf16 v[36:51], " KS(1) ", %[qa], 0\n\t"
                 "v_mfma_f32_32x32x16_bf16 v[52:67], " KS(1) ", %[qb], 0\n\t"
                 "s_nop 15\n\ts_nop 3\n\t"
                 : "=&{v[36:51]}"(sa), "=&{v[52:67]}"(sb)
                 : [lm] "v"(lm), [la] "v"(la), [a0] "v"(lv0), [a1] "v"(lv1), [qa] "v"(qa), [qb] "v"(qb)
                 : "memory", ACLOB);
    const float nm = -halves_max(fmaxf(max16(sa), max16(sb)));
#pragma unroll
    for (int i = 0; i < 16; ++i) mm[i] = nm;
    asm volatile("s_nop 1\n\t" QKA(0) QKB(0) "s_nop 11\n\t"
                 EX4(36) EX4(40) EX4(44) SMF(OA, V0(0), 36) EX4(48) EX4(52) SMF(OA, V1(0), 44) EX4(56) EX4(60) SMF(OB, V0(0), 52) EX4(64)
                 : "=&{v[36:51]}"(sa), "=&{v[52:67]}"(sb)
                 : "{v[68:83]}"(mm), [qa] "v"(qa), [qb] "v"(qb), [idx] "v"(idx) : "memory", ACLOB);
  }
  ST(4);
  ST(12);
  const unsigned kvb = WS_KV + (head * 2 + kh) * 32 * KVB;
  const unsigned tbl = kvb + ((lane + rb) & 31) * KVB;
  unsigned todo = 0xfffffffeu, ready = 0;
  int spins = 0;
  auto pick = [&]() -> unsigned {
    while (!(ready & todo)) {
      if (spins) __builtin_amdgcn_s_sleep(1);
      ready = __builtin_amdgcn_readfirstlane(LDSU(L_CTL));
      if (++spins > (SPIN_MAX << 4)) ready = ~0u;
    }
    const int i = __builtin_ctz(ready & todo);
    todo &= ~(1u << i);
    return __builtin_amdgcn_readlane(tbl, i);
  };
  unsigned s3, s4, s5, s6, s7;
  {
    const unsigned s1 = pick(), s2 = pick();
    s3 = pick(); s4 = pick();
    ST(5);
    asm volatile("s_nop 4\n\t" LDK(1, "%[s1]") LDV(1, "%[s1]") LDK(2, "%[s2]") LDV(2, "%[s2]") LDK(3, "%[s3]") LDV(3, "%[s3]") LDK(0, "%[s4]")
                 "s_waitcnt vmcnt(9)\n\t" QKA(1) "s_nop 15\n\t"
                 : "=&{v[36:51]}"(sa)
                 : "{v[68:83]}"(mm), [qa] "v"(qa), [vk] "v"(vk), [v0] "v"(vv0), [v1] "v"(vv1), [rs] "s"(rs),
                   [s1] "s"(s1), [s2] "s"(s2), [s3] "s"(s3), [s4] "s"(s4)
                 : "memory", ACLOB);
  }
  ST(6);
  __builtin_amdgcn_s_setprio(2);
  s5 = pick(); TILE(1, 0, 2, s4, s5);
  s6 = pick(); TILE(2, 1, 3, s5, s6);
  s7 = pick(); TILE(3, 2, 0, s6, s7);
#pragma unroll 1
  for (int t = 4; t < 28; t += 4) {
    if (t == 20) __builtin_amdgcn_s_setprio(1);
    s4 = pick(); TILE(0, 3, 1, s7, s4);
    s5 = pick(); TILE(1, 0, 2, s4, s5);
    s6 = pick(); TILE(2, 1, 3, s5, s6);
    s7 = pick(); TILE(3, 2, 0, s6, s7);
  }
  TILE(0, 3, 1, s7, s7);
  TILE(1, 0, 2, s7, s7);
  __builtin_amdgcn_s_setprio(0);
  TILE(2, 1, 3, s7, s7);
  TILE(3, 2, 0, s7, s7);
  float oa[9], ob[9];
#define RD(i, r) "v_accvgpr_read_b32 %" STR(i) ", a" STR(r) "\n\t"
  asm volatile("s_nop 1\n\t" SMF(OB, V1(3), 60) "s_waitcnt vmcnt(0)\n\ts_nop 15\n\ts_nop 3\n\t"
               RD(0, 0) RD(1, 1) RD(2, 2) RD(3, 3) RD(4, 4) RD(5, 5) RD(6, 6) RD(7, 7) RD(8, 8)
               RD(9, 16) RD(10, 17) RD(11, 18) RD(12, 19) RD(13, 20) RD(14, 21) RD(15, 22) RD(16, 23) RD(17, 24)
               : "=v"(oa[0]), "=v"(oa[1]), "=v"(oa[2]), "=v"(oa[3]), "=v"(oa[4]), "=v"(oa[5]), "=v"(oa[6]), "=v"(oa[7]), "=v"(oa[8]),
                 "=v"(ob[0]), "=v"(ob[1]), "=v"(ob[2]), "=v"(ob[3]), "=v"(ob[4]), "=v"(ob[5]), "=v"(ob[6]), "=v"(ob[7]), "=v"(ob[8])
               : "{v[52:67]}"(sb), [idx] "v"(idx) : "memory", ACLOB);
  ST(7);
  TAIL(ws, x, out, rs, b, w, oa, ob, qa, qb, vk, vv0, vv1, idx, gate, tlo, thi STPASS);
  STDUMP();
}

extern "C" void kernel_launch(void* const* d_in, const int* in_sizes, int n_in, void* d_out, int out_size, void* d_ws, size_t ws_size,
                              hipStream_t stream) {
  (void)hipFuncSetAttribute((const void*)fused, hipFuncAttributeMaxDynamicSharedMemorySize, LDS_BYTES);
  fused<<<256, 576, LDS_BYTES, stream>>>((const u16*)d_in[0], (const u16*)d_in[1], (const u16*)d_in[2], (const u16*)d_in[4],
                                         (const u16*)d_in[5], (const u16*)d_in[6], (const u16*)d_in[7], (const u16*)d_in[8],
                                         (char*)d_ws, (u16*)d_out);
}
```

```cpp
#define DIAG 99
#define DIAGW 1
#include <hip/hip_runtime.h>
#include <hip/hip_bf16.h>

typedef unsigned short u16;
typedef unsigned int u32;
typedef unsigned long long u64;
typedef __attribute__((ext_vector_type(8))) __bf16 bf16x8;
typedef __attribute__((ext_vector_type(16))) __bf16 bf16x16;
typedef __attribute__((ext_vector_type(16))) float f32x16;
typedef __attribute__((ext_vector_type(8))) float f32x8;
typedef __attribute__((ext_vector_type(4))) float f32x4;
typedef __attribute__((ext_vector_type(4))) unsigned u32x4;
typedef __attribute__((ext_vector_type(2))) unsigned u32x2;
typedef __attribute__((ext_vector_type(4))) short s16x4;
#define LAS __attribute__((address_space(3)))

constexpr int D_ = 512, F_ = 1792;
constexpr u32 KVB = 2176;
constexpr u32 WS_KV = 0;
constexpr u32 WS_OF = 8u << 20;
constexpr u32 WS_FL = 10u << 20;
constexpr u32 WS_OL = WS_FL + 32768;
constexpr u32 WS_H  = WS_OL + 32768;
constexpr u32 WS_Y  = WS_H + 16384;
constexpr u32 WS_EP = WS_Y + 65536;
constexpr u32 WS_END = WS_EP + 128;
constexpr u32 L_X = 0, L_W = 65536, L_KV = 131072, L_WV = 147456, L_QX = 149504, L_RG = 157696, L_ONE = 158208;
constexpr u32 L_GU = 0, L_WD = 16384, L_XS = 23552, L_HS = 24576;
constexpr u32 L_OS = 131072;
constexpr u32 L_CTL = 163808;
constexpr int LDS_BYTES = 163840;
constexpr int SPIN_MAX = 1 << 18;

#define STG(i)
struct Stamps { u64 t0, t, c0; };
#define ST(i) do { if ((i) == 10) { asm volatile("s_memtime %0\n\ts_waitcnt lgkmcnt(0)" : "=s"(stp.c0) :: "memory"); } if ((i) == 11) { asm volatile("s_memtime %0\n\ts_waitcnt lgkmcnt(0)" : "=s"(stp.t) :: "memory"); } } while (0)
#define STDECL
#define STDUMP() do { if (blockIdx.x == 77 && (threadIdx.x >> 6) == DIAGW) { const u64 end = stp.t0 + 6000 + (unsigned)(stp.t - stp.c0) / 5; while (__builtin_amdgcn_s_memrealtime() < end) __builtin_amdgcn_s_sleep(1); } } while (0)
#define STARG , Stamps& stp
#define STPASS , stp

#define HPRIO 3
#define MAP 3
#define MAPB(b, hg, rb) do { if (MAP == 3) { hg = 2 * (b & 3) + ((b >> 3) & 1); rb = 16 * ((b >> 2) & 1) + (b >> 4); } \
  else if (MAP == 2) { hg = 4 * (b & 1) + ((b >> 3) & 3); rb = 8 * ((b >> 1) & 3) + (b >> 5); } \
  else if (MAP == 1) { hg = b & 7; rb = b >> 3; } else { hg = (b >> 3) & 7; rb = 4 * (b & 7) + (b >> 6); } } while (0)
typedef __amdgpu_buffer_rsrc_t rsrc_t;
__device__ __forceinline__ rsrc_t mkrs(const void* p, unsigned bytes) { return __builtin_amdgcn_make_buffer_rsrc((void*)p, 0, (int)bytes, 0x00020000); }
template <unsigned ADD>
__device__ __forceinline__ void dma(unsigned voff, rsrc_t r, unsigned soff, unsigned m) {
  asm volatile("s_add_u32 m0, %0, %c4\n\ts_nop 0\n\tbuffer_load_dwordx4 %1, %2, %3 offen lds" :: "s"(m), "v"(voff), "s"(r), "s"(soff), "i"(ADD) : "memory", "scc");
}
__device__ __forceinline__ unsigned cvtpk(float lo, float hi) {
  typedef __attribute__((ext_vector_type(2))) __bf16 bf16x2;
  typedef __attribute__((ext_vector_type(2))) float f32x2;
  f32x2 v = {lo, hi};
  return __builtin_bit_cast(unsigned, __builtin_convertvector(v, bf16x2));
}
__device__ __forceinline__ float bfround(float f) { return __uint_as_float(cvtpk(f, 0.f) << 16); }
__device__ __forceinline__ float bflo(unsigned u) { return __uint_as_float(u << 16); }
__device__ __forceinline__ float bfhi(unsigned u) { return __uint_as_float(u & 0xffff0000u); }
__device__ __forceinline__ float sigmoidf_(float z) { return __builtin_amdgcn_rcpf(1.f + __expf(-z)); }
__device__ __forceinline__ bf16x8 lds_b128(unsigned a) { return *(const LAS bf16x8*)(size_t)a; }
__device__ __forceinline__ void lds_w128(unsigned a, u32x4 v) { *(LAS u32x4*)(size_t)a = v; }
__device__ __forceinline__ s16x4 lds_tr(unsigned a) { return __builtin_amdgcn_ds_read_tr16_b64_v4i16((LAS s16x4*)(size_t)a); }
__device__ __forceinline__ bf16x8 cat8(s16x4 a, s16x4 b) {
  typedef __attribute__((ext_vector_type(8))) short s16x8;
  s16x8 r = {a[0], a[1], a[2], a[3], b[0], b[1], b[2], b[3]};
  return __builtin_bit_cast(bf16x8, r);
}
__device__ __forceinline__ u32x4 pack8(const f32x16& a, int z, float s) {
  return u32x4{cvtpk(a[8 * z] * s, a[8 * z + 1] * s), cvtpk(a[8 * z + 2] * s, a[8 * z + 3] * s),
               cvtpk(a[8 * z + 4] * s, a[8 * z + 5] * s), cvtpk(a[8 * z + 6] * s, a[8 * z + 7] * s)};
}
__device__ __forceinline__ u32x4 pack8(const f32x16& a, int z) {
  return u32x4{cvtpk(a[8 * z], a[8 * z + 1]), cvtpk(a[8 * z + 2], a[8 * z + 3]), cvtpk(a[8 * z + 4], a[8 * z + 5]), cvtpk(a[8 * z + 6], a[8 * z + 7])};
}
__device__ __forceinline__ unsigned wave_max_u32(unsigned v) {
#pragma unroll
  for (int m = 32; m >= 1; m >>= 1) v = max(v, (unsigned)__shfl_xor((int)v, m));
  return __builtin_amdgcn_readfirstlane(v);
}
#define MFMA32(a, b, c) __builtin_amdgcn_mfma_f32_32x32x16_bf16(a, b, c, 0, 0, 0)
#define MFMA16(a, b, c) __builtin_amdgcn_mfma_f32_16x16x32_bf16(a, b, c, 0, 0, 0)
#define WAIT_BAR(N) asm volatile("s_waitcnt vmcnt(" #N ") lgkmcnt(0)\n\ts_barrier" ::: "memory")
#define BAR() asm volatile("s_waitcnt lgkmcnt(0)\n\ts_barrier" ::: "memory")
#define DRAIN() asm volatile("s_waitcnt vmcnt(0)" ::: "memory")
#define SBAR() __builtin_amdgcn_sched_barrier(0)
#define FRESH() asm volatile("" ::: "memory")
#define LDSU(a) (*(volatile LAS u32*)(size_t)(a))
#define LD128(off) __builtin_amdgcn_raw_buffer_load_b128(rs, (off), 0, 16)
#define LD64(off) __builtin_amdgcn_raw_buffer_load_b64(rs, (off), 0, 16)
#define ST128(v, off) __builtin_amdgcn_raw_buffer_store_b128((v), rs, (off), 0, 16)
#define ST64(v, off) __builtin_amdgcn_raw_buffer_store_b64((v), rs, (off), 0, 16)

#define STR_(x) #x
#define STR(x) STR_(x)
#define EX1(b) "v_exp_f32 v[" STR(b) "], v[" STR(b) "]\n\t"
#define EX4(b) EX1(b) EX1(b+1) EX1(b+2) EX1(b+3)
#define AR4(b) "a[" STR(b) ":" STR(b) "+3]"
#define KS(s) AR4(32+4*s)
#define V0(s) AR4(48+8*s)
#define V1(s) AR4(52+8*s)
#define SMF(acc, va, sb) "v_smfmac_f32_32x32x32_bf16 " acc ", " va ", v[" STR(sb) ":" STR(sb) "+7], %[idx]\n\t"
#define OA "a[0:15]"
#define OB "a[16:31]"
#define QKA(s) "v_mfma_f32_32x32x16_bf16 v[36:51], " KS(s) ", %[qa], v[68:83]\n\t"
#define QKB(s) "v_mfma_f32_32x32x16_bf16 v[52:67], " KS(s) ", %[qb], v[68:83]\n\t"
#define LDK(s, so) "buffer_load_dwordx4 " KS(s) ", %[vk], %[rs], " so " offen sc1\n\t"
#define LDV(s, so) "buffer_load_dwordx4 " V0(s) ", %[v0], %[rs], " so " offen sc1\n\t" \
                   "buffer_load_dwordx4 " V1(s) ", %[v1], %[rs], " so " offen sc1\n\t"
#define ACLOB "a0","a1","a2","a3","a4","a5","a6","a7","a8","a9","a10","a11","a12","a13","a14","a15","a16","a17","a18","a19", \
  "a20","a21","a22","a23","a24","a25","a26","a27","a28","a29","a30","a31","a32","a33","a34","a35","a36","a37","a38","a39", \
  "a40","a41","a42","a43","a44","a45","a46","a47","a48","a49","a50","a51","a52","a53","a54","a55","a56","a57","a58","a59", \
  "a60","a61","a62","a63","a64","a65","a66","a67","a68","a69","a70","a71","a72","a73","a74","a75","a76","a77","a78","a79"
#define TILE(s, sp, sn, soV, soK) \
  asm volatile(EX4(36) SMF(OB, V1(sp), 60) LDV(sp, "%[sv]") EX4(40) QKB(s) LDK(s, "%[sk]") EX4(44) \
               "s_waitcnt vmcnt(9)\n\t" SMF(OA, V0(s), 36) EX4(48) \
               EX4(52) SMF(OA, V1(s), 44) EX4(56) QKA(sn) EX4(60) SMF(OB, V0(s), 52) EX4(64) \
               : "+{v[36:51]}"(sa), "+{v[52:67]}"(sb) \
               : "{v[68:83]}"(mm), [qa] "v"(qa), [qb] "v"(qb), [idx] "v"(idx), [vk] "v"(vk), [v0] "v"(vv0), [v1] "v"(vv1), [rs] "s"(rs), \
                 [sv] "s"(soV), [sk] "s"(soK) : "memory", ACLOB)

__device__ __forceinline__ float max16(const f32x16& p) {
  float a = fmaxf(fmaxf(p[0], p[1]), p[2]), b = fmaxf(fmaxf(p[3], p[4]), p[5]);
  a = fmaxf(fmaxf(a, p[6]), p[7]); b = fmaxf(fmaxf(b, p[8]), p[9]);
  a = fmaxf(fmaxf(a, p[10]), p[11]); b = fmaxf(fmaxf(b, p[12]), p[13]);
  a = fmaxf(fmaxf(a, p[14]), p[15]);
  return fmaxf(a, b);
}
__device__ __forceinline__ float halves_max(float m) {
  auto rr = __builtin_amdgcn_permlane32_swap(__float_as_uint(m), __float_as_uint(m), false, false);
  return fmaxf(__uint_as_float(rr[0]), __uint_as_float(rr[1]));
}

__device__ __forceinline__ void TAIL(char* ws, const u16* x, u16* out, __amdgpu_buffer_rsrc_t rs, int b, int w,
    const float (&oa)[9], const float (&ob)[9], bf16x8 qa, bf16x8 qb, unsigned vk, unsigned vv0, unsigned vv1, unsigned idx,
    float gate, u32 tlo, u32 thi STARG) {
  constexpr unsigned l0 = 0;
  asm volatile("" : "+v"(vk));
  const int lane = vk >> 4;
  int hg, rb; MAPB(b, hg, rb);
  const int c31 = lane & 31, hh = lane >> 5, blk = (lane >> 4) & 1, q4 = (lane & 15) >> 2, p4 = lane & 3;
  const int tt = 2 * rb + (hg >> 2), cg = hg & 3, j = w & 3, kh = w >> 2, head = 4 * hg + j;
#pragma unroll
  for (int i = 0; i < 9; ++i) *(LAS float*)(size_t)(l0 + L_OS + (w ^ 4) * 4096 + i * 256 + lane * 4) = kh ? oa[i] : ob[i];
  unsigned best = 0;
  for (int spin = 0; spin < (SPIN_MAX << 4) && !best; ++spin) best = __builtin_amdgcn_readfirstlane(LDSU(L_CTL + 12));
  best &= 0x7fffffffu;
  BAR();
  float o[9];
  bool bad = false;
#pragma unroll
  for (int i = 0; i < 9; ++i) {
    o[i] = (kh ? ob[i] : oa[i]) + *(const LAS float*)(size_t)(l0 + L_OS + w * 4096 + i * 256 + lane * 4);
    bad |= !(fabsf(o[i]) < 1e30f);
  }
  bad |= !(o[8] > 1e-18f && o[8] < 1e18f);
  if (__any(bad)) {
    const bf16x8 q = kh ? qb : qa;
    const unsigned kb0 = WS_KV + head * 64 * KVB;
    float m = -3.0e38f;
#pragma unroll 1
    for (int t = 0; t < 64; ++t) {
      const f32x16 s = MFMA32(__builtin_bit_cast(bf16x8, LD128(kb0 + t * KVB + vk)), q, (f32x16{0}));
      m = fmaxf(m, max16(s));
    }
    m = -halves_max(m);
    f32x16 c, oc = {0};
#pragma unroll
    for (int i = 0; i < 16; ++i) c[i] = m;
#pragma unroll 1
    for (int t = 0; t < 64; ++t) {
      const f32x16 s = MFMA32(__builtin_bit_cast(bf16x8, LD128(kb0 + t * KVB + vk)), q, c);
      f32x8 e0, e1;
#pragma unroll
      for (int i = 0; i < 8; ++i) { e0[i] = __builtin_amdgcn_exp2f(s[i]); e1[i] = __builtin_amdgcn_exp2f(s[8 + i]); }
      const u32x4 va = __builtin_amdgcn_raw_buffer_load_b128(rs, vv0, kb0 + t * KVB, 16);
      const u32x4 vb = __builtin_amdgcn_raw_buffer_load_b128(rs, vv1, kb0 + t * KVB, 16);
      oc = __builtin_amdgcn_smfmac_f32_32x32x32_bf16(__builtin_bit_cast(bf16x8, va), __builtin_bit_cast(bf16x16, e0), oc, (int)idx, 0, 0);
      oc = __builtin_amdgcn_smfmac_f32_32x32x32_bf16(__builtin_bit_cast(bf16x8, vb), __builtin_bit_cast(bf16x16, e1), oc, (int)idx, 0, 0);
    }
#pragma unroll
    for (int i = 0; i < 9; ++i) o[i] = oc[i];
  }
  {
    const float rl = 1.f / o[8];
    const unsigned lim = 0xfffffffeu;
    ST128((u32x4{min(cvtpk(o[0] * rl, o[1] * rl), lim), min(cvtpk(o[2] * rl, o[3] * rl), lim), min(cvtpk(o[4] * rl, o[5] * rl), lim),
                 min(cvtpk(o[6] * rl, o[7] * rl), lim)}), WS_OF + ((2 * rb + kh) * 32 + head) * 1024 + vk);
  }
  ST(8);

  const int token = tt * 32 + c31, col = cg * 128 + (w & 3) * 32 + hh * 16;
  const int sel = 2047 - (int)(best & 2047u);
  u32x4 xa, xc;
  float yv = 0.f;
  const unsigned abase = (4 * hh + q4) * 256 + (((4 * (w & 3) + 2 * (p4 & 1) + blk) ^ (q4 << 2)) << 4) + 8 * (p4 >> 1);
  if (w < 4) {
    asm volatile(
      "ds_read_b64_tr_b16 a[0:1], %0 offset:0\n\t"
      "ds_read_b64_tr_b16 a[2:3], %0 offset:2048\n\t"
      "ds_read_b64_tr_b16 a[4:5], %0 offset:4096\n\t"
      "ds_read_b64_tr_b16 a[6:7], %0 offset:6144\n\t"
      "ds_read_b64_tr_b16 a[8:9], %0 offset:8192\n\t"
      "ds_read_b64_tr_b16 a[10:11], %0 offset:10240\n\t"
      "ds_read_b64_tr_b16 a[12:13], %0 offset:12288\n\t"
      "ds_read_b64_tr_b16 a[14:15], %0 offset:14336\n\t"
      "ds_read_b64_tr_b16 a[16:17], %0 offset:16384\n\t"
      "ds_read_b64_tr_b16 a[18:19], %0 offset:18432\n\t"
      "ds_read_b64_tr_b16 a[20:21], %0 offset:20480\n\t"
      "ds_read_b64_tr_b16 a[22:23], %0 offset:22528\n\t"
      "ds_read_b64_tr_b16 a[24:25], %0 offset:24576\n\t"
      "ds_read_b64_tr_b16 a[26:27], %0 offset:26624\n\t"
      "ds_read_b64_tr_b16 a[28:29], %0 offset:28672\n\t"
      "ds_read_b64_tr_b16 a[30:31], %0 offset:30720\n\t"
      "ds_read_b64_tr_b16 a[32:33], %0 offset:32768\n\t"
      "ds_read_b64_tr_b16 a[34:35], %0 offset:34816\n\t"
      "ds_read_b64_tr_b16 a[36:37], %0 offset:36864\n\t"
      "ds_read_b64_tr_b16 a[38:39], %0 offset:38912\n\t"
      "ds_read_b64_tr_b16 a[40:41], %0 offset:40960\n\t"
      "ds_read_b64_tr_b16 a[42:43], %0 offset:43008\n\t"
      "ds_read_b64_tr_b16 a[44:45], %0 offset:45056\n\t"
      "ds_read_b64_tr_b16 a[46:47], %0 offset:47104\n\t"
      "ds_read_b64_tr_b16 a[48:49], %0 offset:49152\n\t"
      "ds_read_b64_tr_b16 a[50:51], %0 offset:51200\n\t"
      "ds_read_b64_tr_b16 a[52:53], %0 offset:53248\n\t"
      "ds_read_b64_tr_b16 a[54:55], %0 offset:55296\n\t"
      "ds_read_b64_tr_b16 a[56:57], %0 offset:57344\n\t"
      "ds_read_b64_tr_b16 a[58:59], %0 offset:59392\n\t"
      "ds_read_b64_tr_b16 a[60:61], %0 offset:61440\n\t"
      "ds_read_b64_tr_b16 a[62:63], %0 offset:63488\n\t"
      "ds_read_b64_tr_b16 a[64:65], %1 offset:0\n\t"
      "ds_read_b64_tr_b16 a[66:67], %1 offset:2048\n\t"
      "ds_read_b64_tr_b16 a[68:69], %1 offset:4096\n\t"
      "ds_read_b64_tr_b16 a[70:71], %1 offset:6144\n\t"
      "ds_read_b64_tr_b16 a[72:73], %1 offset:8192\n\t"
      "ds_read_b64_tr_b16 a[74:75], %1 offset:10240\n\t"
      "ds_read_b64_tr_b16 a[76:77], %1 offset:12288\n\t"
      "ds_read_b64_tr_b16 a[78:79], %1 offset:14336\n\t"
      :: "v"(abase), "v"(abase + 65536) : "memory", ACLOB);
    xa = *(const u32x4*)(x + (size_t)token * D_ + col); xc = *(const u32x4*)(x + (size_t)token * D_ + col + 8);
    if ((sel >> 5) == tt) {
      for (int spin = 0; spin < SPIN_MAX; ++spin) {
      if (spin) __builtin_amdgcn_s_sleep(4);
      FRESH();
        bool ok = true; yv = 0.f;
#pragma unroll
        for (int i = 0; i < 4; ++i) {
          const u32x4 a = LD128(WS_Y + ((4 * hh + i) * 512 + cg * 128 + w * 32 + c31) * 16);
          ok &= a[1] == thi && a[3] == thi && ((a[0] ^ tlo) | (a[2] ^ tlo)) < 0x10000u;
          yv += __uint_as_float((a[0] & 0xffffu) | (a[2] << 16));
        }
        if (__all(ok)) break;
      }
      yv += __shfl_xor(yv, 32);
    }
  }
  float tb[16];
  if (w < 4) {
#pragma unroll
    for (int i = 0; i < 4; ++i) { tb[2 * i] = bflo(xa[i]); tb[2 * i + 1] = bfhi(xa[i]); tb[8 + 2 * i] = bflo(xc[i]); tb[9 + 2 * i] = bfhi(xc[i]); }
    if ((sel >> 5) == tt) {
      const unsigned srt = best >> 11;
      const float rsel = bfround(sigmoidf_(bflo((srt & 0x8000u) ? (srt & 0x7fffu) : (~srt & 0xffffu))));
      const float yy = bfround(bfround(yv) * rsel);
#pragma unroll
      for (int i = 0; i < 16; ++i) { const float t = __shfl(yy, hh * 16 + i); if (token == sel) tb[i] = bfround(tb[i] + t); }
    }
    const float og = bfround(1.f - gate);
#pragma unroll
    for (int i = 0; i < 16; ++i) tb[i] *= og;
  }
  {
    u32x4 v[4];
    for (int spin = 0; spin < SPIN_MAX; ++spin) {
      if (spin) __builtin_amdgcn_s_sleep(4);
      FRESH();
      unsigned m = 0;
#pragma unroll
      for (int i = 0; i < 4; ++i) v[i] = LD128(WS_OF + (tt * 32 + 4 * w + i) * 1024 + vk);
#pragma unroll
      for (int i = 0; i < 4; ++i) m = max(max(m, max(v[i][0], v[i][1])), max(v[i][2], v[i][3]));
      if (!__any(m == ~0u)) break;
    }
    ST(9);
#pragma unroll
    for (int i = 0; i < 4; ++i) lds_w128(l0 + L_OS + (4 * w + i) * 1024 + vk, v[i]);
  }
  BAR();
  ST(10);
  if (w < 4) {
    f32x16 a;
    asm volatile(
      "ds_read_b128 v[20:23], %[b] offset:0\n\t"
      "ds_read_b128 v[24:27], %[b] offset:1024\n\t"
      "ds_read_b128 v[28:31], %[b] offset:2048\n\t"
      "ds_read_b128 v[32:35], %[b] offset:3072\n\t"
      "ds_read_b128 v[36:39], %[b] offset:4096\n\t"
      "ds_read_b128 v[40:43], %[b] offset:5120\n\t"
      "ds_read_b128 v[44:47], %[b] offset:6144\n\t"
      "ds_read_b128 v[48:51], %[b] offset:7168\n\t"
      "s_waitcnt lgkmcnt(7)\n\t"
      "v_mfma_f32_32x32x16_bf16 v[68:83], a[0:3], v[20:23], 0\n\t"
      "ds_read_b128 v[20:23], %[b] offset:8192\n\t"
      "ds_read_b64_tr_b16 a[0:1], %[a1] offset:16384\n\t"
      "ds_read_b64_tr_b16 a[2:3], %[a1] offset:18432\n\t"
      "s_waitcnt lgkmcnt(9)\n\t"
      "v_mfma_f32_32x32x16_bf16 v[68:83], a[4:7], v[24:27], v[68:83]\n\t"
      "ds_read_b128 v[24:27], %[b] offset:9216\n\t"
      "ds_read_b64_tr_b16 a[4:5], %[a1] offset:20480\n\t"
      "ds_read_b64_tr_b16 a[6:7], %[a1] offset:22528\n\t"
      "s_waitcnt lgkmcnt(11)\n\t"
      "v_mfma_f32_32x32x16_bf16 v[68:83], a[8:11], v[28:31], v[68:83]\n\t"
      "ds_read_b128 v[28:31], %[b] offset:10240\n\t"
      "ds_read_b64_tr_b16 a[8:9], %[a1] offset:24576\n\t"
      "ds_read_b64_tr_b16 a[10:11], %[a1] offset:26624\n\t"
      "s_waitcnt lgkmcnt(13)\n\t"
      "v_mfma_f32_32x32x16_bf16 v[68:83], a[12:15], v[32:35], v[68:83]\n\t"
      "ds_read_b128 v[32:35], %[b] offset:11264\n\t"
      "ds_read_b64_tr_b16 a[12:13], %[a1] offset:28672\n\t"
      "ds_read_b64_tr_b16 a[14:15], %[a1] offset:30720\n\t"
      "s_waitcnt lgkmcnt(15)\n\t"
      "v_mfma_f32_32x32x16_bf16 v[68:83], a[16:19], v[36:39], v[68:83]\n\t"
      "ds_read_b128 v[36:39], %[b] offset:12288\n\t"
      "ds_read_b64_tr_b16 a[16:17], %[a1] offset:32768\n\t"
      "ds_read_b64_tr_b16 a[18:19], %[a1] offset:34816\n\t"
      "s_waitcnt lgkmcnt(15)\n\t"
      "v_mfma_f32_32x32x16_bf16 v[68:83], a[20:23], v[40:43], v[68:83]\n\t"
      "ds_read_b128 v[40:43], %[b] offset:13312\n\t"
      "ds_read_b64_tr_b16 a[20:21], %[a1] offset:36864\n\t"
      "ds_read_b64_tr_b16 a[22:23], %[a1] offset:38912\n\t"
      "s_waitcnt lgkmcnt(15)\n\t"
      "v_mfma_f32_32x32x16_bf16 v[68:83], a[24:27], v[44:47], v[68:83]\n\t"
      "ds_read_b128 v[44:47], %[b] offset:14336\n\t"
      "ds_read_b64_tr_b16 a[24:25], %[a1] offset:40960\n\t"
      "ds_read_b64_tr_b16 a[26:27], %[a1] offset:43008\n\t"
      "s_waitcnt lgkmcnt(15)\n\t"
      "v_mfma_f32_32x32x16_bf16 v[68:83], a[28:31], v[48:51], v[68:83]\n\t"
      "ds_read_b128 v[48:51], %[b] offset:15360\n\t"
      "ds_read_b64_tr_b16 a[28:29], %[a1] offset:45056\n\t"
      "ds_read_b64_tr_b16 a[30:31], %[a1] offset:47104\n\t"
      "s_waitcnt lgkmcnt(15)\n\t"
      "v_mfma_f32_32x32x16_bf16 v[68:83], a[32:35], v[20:23], v[68:83]\n\t"
      "ds_read_b128 v[20:23], %[b] offset:16384\n\t"
      "ds_read_b64_tr_b16 a[32:33], %[a1] offset:49152\n\t"
      "ds_read_b64_tr_b16 a[34:35], %[a1] offset:51200\n\t"
      "s_waitcnt lgkmcnt(15)\n\t"
      "v_mfma_f32_32x32x16_bf16 v[68:83], a[36:39], v[24:27], v[68:83]\n\t"
      "ds_read_b128 v[24:27], %[b] offset:17408\n\t"
      "ds_read_b64_tr_b16 a[36:37], %[a1] offset:53248\n\t"
      "ds_read_b64_tr_b16 a[38:39], %[a1] offset:55296\n\t"
      "s_waitcnt lgkmcnt(15)\n\t"
      "v_mfma_f32_32x32x16_bf16 v[68:83], a[40:43], v[28:31], v[68:83]\n\t"
      "ds_read_b128 v[28:31], %[b] offset:18432\n\t"
      "ds_read_b64_tr_b16 a[40:41], %[a1] offset:57344\n\t"
      "ds_read_b64_tr_b16 a[42:43], %[a1] offset:59392\n\t"
      "s_waitcnt lgkmcnt(15)\n\t"
      "v_mfma_f32_32x32x16_bf16 v[68:83], a[44:47], v[32:35], v[68:83]\n\t"
      "ds_read_b128 v[32:35], %[b] offset:19456\n\t"
      "ds_read_b64_tr_b16 a[44:45], %[a1] offset:61440\n\t"
      "ds_read_b64_tr_b16 a[46:47], %[a1] offset:63488\n\t"
      "s_waitcnt lgkmcnt(15)\n\t"
      "v_mfma_f32_32x32x16_bf16 v[68:83], a[48:51], v[36:39], v[68:83]\n\t"
      "ds_read_b128 v[36:39], %[b] offset:20480\n\t"
      "s_waitcnt lgkmcnt(15)\n\t"
      "v_mfma_f32_32x32x16_bf16 v[68:83], a[52:55], v[40:43], v[68:83]\n\t"
      "ds_read_b128 v[40:43], %[b] offset:21504\n\t"
      "s_waitcnt lgkmcnt(15)\n\t"
      "v_mfma_f32_32x32x16_bf16 v[68:83], a[56:59], v[44:47], v[68:83]\n\t"
      "ds_read_b128 v[44:47], %[b] offset:22528\n\t"
      "s_waitcnt lgkmcnt(15)\n\t"
      "v_mfma_f32_32x32x16_bf16 v[68:83], a[60:63], v[48:51], v[68:83]\n\t"
      "ds_read_b128 v[48:51], %[b] offset:23552\n\t"
      "s_waitcnt lgkmcnt(15)\n\t"
      "v_mfma_f32_32x32x16_bf16 v[68:83], a[64:67], v[20:23], v[68:83]\n\t"
      "ds_read_b128 v[20:23], %[b] offset:24576\n\t"
      "s_waitcnt lgkmcnt(13)\n\t"
      "v_mfma_f32_32x32x16_bf16 v[68:83], a[68:71], v[24:27], v[68:83]\n\t"
      "ds_read_b128 v[24:27], %[b] offset:25600\n\t"
      "s_waitcnt lgkmcnt(11)\n\t"
      "v_mfma_f32_32x32x16_bf16 v[68:83], a[72:75], v[28:31], v[68:83]\n\t"
      "ds_read_b128 v[28:31], %[b] offset:26624\n\t"
      "s_waitcnt lgkmcnt(9)\n\t"
      "v_mfma_f32_32x32x16_bf16 v[68:83], a[76:79], v[32:35], v[68:83]\n\t"
      "ds_read_b128 v[32:35], %[b] offset:27648\n\t"
      "s_waitcnt lgkmcnt(7)\n\t"
      "v_mfma_f32_32x32x16_bf16 v[68:83], a[0:3], v[36:39], v[68:83]\n\t"
      "ds_read_b128 v[36:39], %[b] offset:28672\n\t"
      "s_waitcnt lgkmcnt(7)\n\t"
      "v_mfma_f32_32x32x16_bf16 v[68:83], a[4:7], v[40:43], v[68:83]\n\t"
      "ds_read_b128 v[40:43], %[b] offset:29696\n\t"
      "s_waitcnt lgkmcnt(7)\n\t"
      "v_mfma_f32_32x32x16_bf16 v[68:83], a[8:11], v[44:47], v[68:83]\n\t"
      "ds_read_b128 v[44:47], %[b] offset:30720\n\t"
      "s_waitcnt lgkmcnt(7)\n\t"
      "v_mfma_f32_32x32x16_bf16 v[68:83], a[12:15], v[48:51], v[68:83]\n\t"
      "ds_read_b128 v[48:51], %[b] offset:31744\n\t"
      "s_waitcnt lgkmcnt(7)\n\t"
      "v_mfma_f32_32x32x16_bf16 v[68:83], a[16:19], v[20:23], v[68:83]\n\t"
      "s_waitcnt lgkmcnt(6)\n\t"
      "v_mfma_f32_32x32x16_bf16 v[68:83], a[20:23], v[24:27], v[68:83]\n\t"
      "s_waitcnt lgkmcnt(5)\n\t"
      "v_mfma_f32_32x32x16_bf16 v[68:83], a[24:27], v[28:31], v[68:83]\n\t"
      "s_waitcnt lgkmcnt(4)\n\t"
      "v_mfma_f32_32x32x16_bf16 v[68:83], a[28:31], v[32:35], v[68:83]\n\t"
      "s_waitcnt lgkmcnt(3)\n\t"
      "v_mfma_f32_32x32x16_bf16 v[68:83], a[32:35], v[36:39], v[68:83]\n\t"
      "s_waitcnt lgkmcnt(2)\n\t"
      "v_mfma_f32_32x32x16_bf16 v[68:83], a[36:39], v[40:43], v[68:83]\n\t"
      "s_waitcnt lgkmcnt(1)\n\t"
      "v_mfma_f32_32x32x16_bf16 v[68:83], a[40:43], v[44:47], v[68:83]\n\t"
      "s_waitcnt lgkmcnt(0)\n\t"
      "v_mfma_f32_32x32x16_bf16 v[68:83], a[44:47], v[48:51], v[68:83]\n\t"
      "s_nop 15\n\t"
      "s_nop 3\n\t"
      : "=&{v[68:83]}"(a) : [b] "v"(L_OS + vk), [a1] "v"(abase + 65536) : "memory", ACLOB, "v20","v21","v22","v23","v24","v25","v26","v27","v28","v29","v30","v31","v32","v33","v34","v35","v36","v37","v38","v39","v40","v41","v42","v43","v44","v45","v46","v47","v48","v49","v50","v51");
    float r[16];
#pragma unroll
    for (int i = 0; i < 16; ++i) r[i] = fmaf(gate, a[i], tb[i]);
    *(u32x4*)(out + (size_t)token * D_ + col) = u32x4{cvtpk(r[0], r[1]), cvtpk(r[2], r[3]), cvtpk(r[4], r[5]), cvtpk(r[6], r[7])};
    *(u32x4*)(out + (size_t)token * D_ + col + 8) = u32x4{cvtpk(r[8], r[9]), cvtpk(r[10], r[11]), cvtpk(r[12], r[13]), cvtpk(r[14], r[15])};
    if (b == 0 && w == 0 && lane == 0) ST64((u32x2{tlo, thi}), WS_EP);
  }
  ST(11);
}

__global__ void __launch_bounds__(576, 3)
fused(const u16* __restrict__ x, const u16* __restrict__ W, const u16* __restrict__ wo, const u16* __restrict__ wr,
      const u16* __restrict__ wg, const u16* __restrict__ wu, const u16* __restrict__ wd, const u16* __restrict__ gw,
      char* __restrict__ ws, u16* __restrict__ out) {
  Stamps stp; asm volatile("s_memrealtime %0\n\ts_waitcnt lgkmcnt(0)" : "=s"(stp.t0) :: "memory"); stp.t = stp.t0; stp.c0 = stp.t0;
  constexpr unsigned l0 = 0;
  const int b = blockIdx.x;
  int hg, rb; MAPB(b, hg, rb);
  const int lane = threadIdx.x & 63, w = __builtin_amdgcn_readfirstlane(threadIdx.x >> 6);
  const int c31 = lane & 31, hh = lane >> 5, blk = (lane >> 4) & 1, q4 = (lane & 15) >> 2, p4 = lane & 3;
  const int tt = 2 * rb + (hg >> 2), cg = hg & 3;
  const __amdgpu_buffer_rsrc_t rs = __builtin_amdgcn_make_buffer_rsrc(ws, 0, (int)WS_END, 0x00020000);
  STDECL
  ST(0);
  asm volatile("" :: "s"(x), "s"(W), "s"(wr), "s"(gw), "s"(ws));
#define GET_TAG() u64 ep_; asm volatile("s_load_dwordx2 %0, %1, 0x0\n\ts_waitcnt lgkmcnt(0)" : "=s"(ep_) : "s"(ws + WS_EP) : "memory"); \
  const u64 tag = ep_ * 0x9E3779B97F4A7C15ull + 0xD1B54A32D192ED03ull; const u32 thi = (u32)(tag >> 32), tlo = (u32)tag

  if (w == 8) {
#pragma unroll
    for (int i = 0; i < 8; ++i) ST128((u32x4{~0u, ~0u, ~0u, ~0u}), WS_OF + ((2 * rb + (i >> 2)) * 32 + 4 * hg + (i & 3)) * 1024 + lane * 16);
    if (lane < 4) LDSU(L_CTL + lane * 4) = 0;
    GET_TAG();
#pragma unroll 1
    for (int i = 0; i < 6; ++i) BAR();
    DRAIN();
    if (lane == 0) ST64((u32x2{tlo, thi}), WS_OL + (rb * 8 + hg) * 128);
#pragma unroll 1
    for (int i = 0; i < 5; ++i) BAR();
    const int cg1 = (b & 7) * 28 + (b >> 3);
    const int ks2 = b & 7, cg2 = b >> 3;
    const bool has1 = (b >> 3) < 28;
    ST(2);
    __builtin_amdgcn_s_setprio(HPRIO);
    const rsrc_t ro = mkrs(wo, D_ * D_ * 2);
    const unsigned vwo = (lane >> 4) * (D_ * 2) + cg * 256 + (((lane & 15) ^ ((lane >> 4) << 2)) * 16);
    unsigned key = 0;
    for (int spin = 0; spin < SPIN_MAX; ++spin) {
      if (spin) __builtin_amdgcn_s_sleep(2);
      FRESH();
      const u32x2 f = LD64(WS_FL + (hg * 32 + ((lane + rb) & 31)) * 128);
      key = f[0] & 0x7ffffffu;
      const unsigned rdy = (unsigned)__ballot(f[1] == thi && (f[0] ^ tlo) < 0x8000000u);
      if (lane == 0) LDSU(L_CTL) = rdy;
      if (rdy == ~0u) break;
    }
    if (lane == 0) LDSU(L_CTL) = ~0u;
    ST(3);
    const unsigned best = wave_max_u32(key);
    const int sel = 2047 - (int)(best & 2047u);
    if (lane == 0) LDSU(L_CTL + 12) = best | 0x80000000u;
    if (has1) {
      const rsrc_t rg = mkrs(wg, D_ * F_ * 2), ru = mkrs(wu, D_ * F_ * 2);
      const unsigned vo = (lane >> 1) * (F_ * 2) + cg1 * 16;
#pragma unroll 1
      for (int i = 0; i < 16; ++i)
        asm volatile("s_mov_b32 m0, %0\n\ts_mov_b64 exec, %5\n\tbuffer_load_dwordx4 %1, %2, %4 offen lds\n\ts_not_b64 exec, exec\n\t"
                     "buffer_load_dwordx4 %1, %3, %4 offen lds\n\ts_mov_b64 exec, -1"
                     :: "s"(L_GU + i * 1024), "v"(vo), "s"(rg), "s"(ru), "s"(i * 32 * F_ * 2), "s"(0x5555555555555555ull) : "memory", "scc");
    }
    {
      const rsrc_t rd = mkrs(wd, D_ * F_ * 2);
      const unsigned vo = (ks2 * 224 + (lane >> 1)) * (D_ * 2) + cg2 * 32 + (lane & 1) * 16;
#pragma unroll 1
      for (int i = 0; i < 7; ++i) dma<L_WD>(vo, rd, i * 32 * D_ * 2, i * 1024);
    }
    if (has1) {
      dma<L_XS>(lane * 16, mkrs(x, 2048 * D_ * 2), sel * (D_ * 2), 0);
      ST(4);
      DRAIN();
      ST(5);
      f32x4 d = {0};
      const unsigned ba = l0 + L_GU + (8 * (lane >> 4) + q4) * 32 + p4 * 8;
#pragma unroll 4
      for (int u = 0; u < 16; ++u)
        d = MFMA16(lds_b128(l0 + L_XS + (4 * u + (lane >> 4)) * 16), cat8(lds_tr(ba + u * 1024), lds_tr(ba + u * 1024 + 128)), d);
      const float gp = bfround(d[0]), up = bfround(__shfl_down(d[0], 8));
      const unsigned hb = cvtpk(bfround(gp * sigmoidf_(gp)) * up, 0.f) & 0xffffu;
      if (lane < 8) ST64((u32x2{(tlo & 0xffff0000u) | hb, thi}), WS_H + (cg1 * 8 + lane) * 8);
    }
    ST(6);
#pragma unroll 1
    for (int i = 25; i < 80; ++i) dma<0>(vwo, ro, i * 4096, i * 1024);
    ST(7);
    for (int spin = 0; spin < SPIN_MAX; ++spin) {
      if (spin) __builtin_amdgcn_s_sleep(4);
      FRESH();
      const unsigned o = WS_H + ks2 * 1792 + (lane < 56 ? lane : 0) * 32;
      const u32x4 a = LD128(o), c = LD128(o + 16);
      const bool ok = a[1] == thi && a[3] == thi && c[1] == thi && c[3] == thi &&
                      ((a[0] ^ tlo) | (a[2] ^ tlo) | (c[0] ^ tlo) | (c[2] ^ tlo)) < 0x10000u;
      *(LAS u32x2*)(size_t)(l0 + L_HS + lane * 8) = u32x2{(a[0] & 0xffffu) | (a[2] << 16), (c[0] & 0xffffu) | (c[2] << 16)};
      if (__all(ok)) break;
    }
    ST(8);
    {
      f32x4 d = {0};
      const unsigned ba = l0 + L_WD + (8 * (lane >> 4) + q4) * 32 + p4 * 8;
#pragma unroll
      for (int u = 0; u < 7; ++u)
        d = MFMA16(lds_b128(l0 + L_HS + (4 * u + (lane >> 4)) * 16), cat8(lds_tr(ba + u * 1024), lds_tr(ba + u * 1024 + 128)), d);
      const unsigned yb = __float_as_uint(d[0]);
      if (lane < 16) ST128((u32x4{(tlo & 0xffff0000u) | (yb & 0xffffu), thi, (tlo & 0xffff0000u) | (yb >> 16), thi}),
                           WS_Y + (ks2 * 512 + cg2 * 16 + lane) * 16);
      asm volatile("s_waitcnt lgkmcnt(0)" ::: "memory");
    }
    ST(9);
#pragma unroll 1
    for (int i = 80; i < 128 + 25; ++i) { const int r = i & 127; dma<0>(vwo, ro, r * 4096, r * 1024); }
    for (int spin = 0; spin < SPIN_MAX; ++spin) {
      if (spin) __builtin_amdgcn_s_sleep(4);
      FRESH();
      const u32x2 f = LD64(WS_OL + (rb * 8 + (lane & 7)) * 128);
      if (__all(f[0] == tlo && f[1] == thi)) break;
    }
    ST(1);
    WAIT_BAR(0);
    BAR();
    STDUMP();
    return;
  }

  const int rot = (rb + hg) & 7;
  const rsrc_t rx = mkrs(x, 2048 * D_ * 2), rw = mkrs(W, D_ * 1536 * 2);
  const unsigned mw = w * 1024;
  const unsigned vx = (rb * 64 + w * 8 + (lane >> 3)) * 1024 + (((lane & 7) ^ ((w * 4 + (lane >> 4)) & 7)) << 4);
  const unsigned vq = ((lane >> 3) + w * 8) * 3072 + hg * 128 + (((lane & 7) ^ (((lane >> 4) & 1) << 2)) << 4);
  const unsigned vkv = vq + (w < 4 ? 1024 : 2048 - 32 * 3072);
  if (w == 4) dma<L_WV>(((lane + 8 * rot) & 63) * 16, mkrs(wr, 1024), 0, 0);
  if (w == 5) dma<L_WV + 1024>(((lane + 8 * rot) & 63) * 16, mkrs(gw, 1024), 0, 0);
#define KBLK(g) ((unsigned)(((g) + rot) & 7))
#define ISSX(g) dma<L_X + (g) * 8192>(vx, rx, KBLK(g) * 128, mw)
#define ISSUE(r) do { if ((r) < 16) dma<L_W + (((r) + 4) % 10) * 8192>(vkv, rw, KBLK((r) >> 1) * 196608 + ((r) & 1) * 98304, mw); \
                      else dma<L_W + (((r) + 4) % 10) * 8192>(vq, rw, KBLK(r) * 196608, mw); } while (0)
#define ISSG(g) do { ISSX(g); ISSUE(2 * (g)); ISSUE(2 * (g) + 1); } while (0)
#define LEAD 4
  ISSG(0); ISSG(1); ISSG(2);
  if (LEAD == 4) ISSG(3);

  const int part = w >> 2, pair = w & 1, tile = (w >> 1) & 1;
  unsigned xa0 = L_X + (tile * 32 + c31) * 128 + ((hh ^ ((c31 >> 1) & 7)) << 4);
  unsigned xa1 = xa0 ^ 32, xa2 = xa0 ^ 64, xa3 = xa0 ^ 96;
  unsigned wq0 = L_W + (8 * hh + q4) * 128 + (((4 * pair + 2 * blk + (p4 >> 1)) ^ (((q4 >> 1) & 1) << 2)) << 4) + 8 * (p4 & 1);
  unsigned wa0 = wq0 + part * 4096, wa1 = wa0 + 65536;
  asm volatile("" : "+v"(xa0), "+v"(xa1), "+v"(xa2), "+v"(xa3), "+v"(wa0), "+v"(wa1), "+v"(wq0));
  f32x16 acc = {0};
  GET_TAG();
  {
    bf16x8 wf[2][4], xf[2][4];
#define WADDR(g, u) ((((2 * (g) + 4 + ((u) >> 1)) % 10) >= 8 ? wa1 - 65536 : wa0) + ((2 * (g) + 4 + ((u) >> 1)) % 10) * 8192 + ((u) & 1) * 2048)
#define RD1(g, u, xa) do { xf[(g) & 1][u] = lds_b128(xa + (g) * 8192); \
      wf[(g) & 1][u] = cat8(lds_tr(WADDR(g, u)), lds_tr(WADDR(g, u) + 512)); } while (0)
#define RD4(g) do { RD1(g, 0, xa0); RD1(g, 1, xa1); RD1(g, 2, xa2); RD1(g, 3, xa3); } while (0)
#define MM(V, g, u) do { if (V) acc = MFMA32(xf[(g) & 1][u], wf[(g) & 1][u], acc); else acc = MFMA32(wf[(g) & 1][u], xf[(g) & 1][u], acc); SBAR(); } while (0)
#define NXT(g, i) do { if ((g) + LEAD <= 7) { if ((i) == 0) ISSX(((g) + LEAD) & 7); else ISSUE(2 * ((g) + LEAD) + (i) - 1); } \
      if ((g) >= 4 && (i) > 0) ISSUE(2 * (g) + 7 + (i)); } while (0)
#define GROUP(V, g, N) do { WAIT_BAR(N); STG(4 + (g)); MM(V, (g) - 1, 0); RD1(g, 0, xa0); NXT(g, 0); SBAR(); MM(V, (g) - 1, 1); RD1(g, 1, xa1); NXT(g, 1); SBAR(); \
      MM(V, (g) - 1, 2); RD1(g, 2, xa2); NXT(g, 2); SBAR(); MM(V, (g) - 1, 3); RD1(g, 3, xa3); SBAR(); } while (0)
#define WB0 9
#define KVLOOP_(V, N) do { WAIT_BAR(N); ST(1); RD4(0); NXT(0, 0); NXT(0, 1); NXT(0, 2); SBAR(); GROUP(V, 1, N); GROUP(V, 2, N); GROUP(V, 3, N); GROUP(V, 4, N); \
      GROUP(V, 5, 8); GROUP(V, 6, 7); if (LEAD == 4) GROUP(V, 7, 6); else GROUP(V, 7, 5); MM(V, 7, 0); MM(V, 7, 1); MM(V, 7, 2); MM(V, 7, 3); } while (0)
#define KVLOOP__(V, N) KVLOOP_(V, N)
#define KVLOOP(V) KVLOOP__(V, WB0)
    if (part == 0) KVLOOP(false); else KVLOOP(true);
  }
  ST(2);
  const u32x4 p0 = pack8(acc, 0), p1 = pack8(acc, 1);
  unsigned o0, o1;
  int h0, h1;
  if (part == 0) {
    const int s23 = (c31 & 19) | ((c31 & 4) << 1) | ((c31 & 8) >> 1);
    o0 = o1 = (s23 + 32 * hh) * 16; h0 = 2 * pair; h1 = 2 * pair + 1;
  } else {
    o0 = 1024 + (hh * 16 + (lane & 15)) * 16; o1 = o0 + 512; h0 = h1 = 2 * pair + blk;
  }
  ST128(p0, WS_KV + (((4 * hg + h0) * 2 + tile) * 32 + rb) * KVB + o0);
  ST128(p1, WS_KV + (((4 * hg + h1) * 2 + tile) * 32 + rb) * KVB + o1);
  if (part == 1 && lane < 2) ST128((u32x4{0x3F803F80u, 0x3F803F80u, 0x3F803F80u, 0x3F803F80u}), WS_KV + (((4 * hg + 2 * pair + lane) * 2 + tile) * 32 + rb) * KVB + 2048);
  if (part == 0) WAIT_BAR(2); else WAIT_BAR(3);
  ST(13);
  lds_w128(l0 + L_KV + (h0 * 2 + tile) * 2048 + o0, p0);
  lds_w128(l0 + L_KV + (h1 * 2 + tile) * 2048 + o1, p1);
  if (part == 0) {
    f32x16 qacc = {0};
    bf16x8 wf[2][4], xf[2][4];
#define QR1(r, u, xa) do { xf[(r) & 1][u] = lds_b128(xa + (r) * 8192); \
      wf[(r) & 1][u] = cat8(lds_tr(wq0 + (r) * 8192 + (u) * 2048), lds_tr(wq0 + (r) * 8192 + (u) * 2048 + 512)); } while (0)
#define QR4(r) do { QR1(r, 0, xa0); QR1(r, 1, xa1); QR1(r, 2, xa2); QR1(r, 3, xa3); } while (0)
    QR4(0);
    SBAR();
#define QS(r, u, xa) do { qacc = MFMA32(wf[(r) & 1][u], xf[(r) & 1][u], qacc); SBAR(); if ((r) < 7) QR1((r) + 1, u, xa); SBAR(); } while (0)
#define QG(r) do { QS(r, 0, xa0); QS(r, 1, xa1); QS(r, 2, xa2); QS(r, 3, xa3); } while (0)
    QG(0); QG(1); QG(2); QG(3); QG(4); QG(5); QG(6); QG(7);
    const float qs = 0.25f * 1.4426950408889634f;
    lds_w128(l0 + L_QX + ((2 * pair) * 2 + tile) * 1024 + lane * 16, pack8(qacc, 0, qs));
    lds_w128(l0 + L_QX + ((2 * pair + 1) * 2 + tile) * 1024 + lane * 16, pack8(qacc, 1, qs));
  } else {
    const int tok = 16 * (w & 3) + (lane & 15), g4 = lane >> 4;
    f32x4 d = {0};
    unsigned fa0 = L_WV + (lane & 1) * 1024 + g4 * 16, fb0 = L_X + tok * 128 + ((g4 ^ ((tok >> 1) & 7)) << 4), fb1 = fb0 ^ 64;
    asm volatile("" : "+v"(fa0), "+v"(fb0), "+v"(fb1));
    bf16x8 fa[16], fb[16];
#define RGR(u) do { fa[u] = lds_b128(fa0 + (u) * 64); fb[u] = lds_b128((((u) & 1) ? fb1 : fb0) + ((u) >> 1) * 8192); } while (0)
    RGR(0); RGR(1); RGR(2); RGR(3);
    SBAR();
#pragma unroll
    for (int u = 0; u < 16; ++u) { d = MFMA16(fa[u], fb[u], d); SBAR(); if (u + 4 < 16) RGR((u + 4) & 15); SBAR(); }
    const unsigned r16 = cvtpk(d[0], 0.f) & 0xffffu;
    unsigned key = (((r16 & 0x8000u) ? (~r16 & 0xffffu) : (r16 | 0x8000u)) << 11) | (2047 - (rb * 64 + tok));
    if (lane < 16) {
      asm volatile("ds_max_u32 %0, %1" :: "v"(L_CTL + 8), "v"(key) : "memory");
      *(LAS float*)(size_t)(l0 + L_RG + tok * 4) = bfround(sigmoidf_(bfround(d[1])));
    }
    if (w == 7 && lane < 2) { const unsigned v = lane ? 0u : 0x3F803F80u; lds_w128(l0 + L_ONE + lane * 16, u32x4{v, v, v, v}); }
  }
  ST(3);
  BAR();
  DRAIN();
  if (lane == 0 && __hip_atomic_fetch_add((LAS u32*)(size_t)(L_CTL + 4), 1u, __ATOMIC_RELAXED, __HIP_MEMORY_SCOPE_WORKGROUP) == 7)
    ST64((u32x2{(tlo & 0xf8000000u) | LDSU(L_CTL + 8), thi}), WS_FL + (hg * 32 + rb) * 128);
  ST(14);

  const int j = w & 3, kh = w >> 2, head = 4 * hg + j;
  const bool onerow = c31 == 16 || c31 == 20;
  const unsigned idx = 0xDDDDDDDDu, vk = lane * 16;
  const unsigned vv0 = c31 < 16 ? 1024 + (hh * 16 + c31) * 16 : (onerow ? 2048u : 0x7f000000u);
  const unsigned vv1 = c31 < 16 ? vv0 + 512 : vv0;
  const bf16x8 qa = lds_b128(l0 + L_QX + (j * 2) * 1024 + lane * 16), qb = lds_b128(l0 + L_QX + (j * 2 + 1) * 1024 + lane * 16);
  const float gate = *(const LAS float*)(size_t)(l0 + L_RG + ((hg >> 2) * 32 + c31) * 4);
  f32x16 sa, sb, mm;
  {
    const unsigned lk = l0 + L_KV + (j * 2 + kh) * 2048;
    const unsigned la = lk + vk, lm = l0 + L_KV + j * 4096 + vk;
    const unsigned lv0 = c31 < 16 ? lk + vv0 : l0 + L_ONE + (onerow ? 0 : 16), lv1 = c31 < 16 ? lv0 + 512 : lv0;
#define Z1(r) "v_accvgpr_write_b32 a[" STR(r) "], 0\n\t"
#define Z4(r) Z1(r) Z1(r+1) Z1(r+2) Z1(r+3)
#define Z16(r) Z4(r) Z4(r+4) Z4(r+8) Z4(r+12)
    asm volatile("ds_read_b128 " KS(1) ", %[lm]\n\tds_read_b128 " KS(0) ", %[la]\n\tds_read_b128 " V0(0) ", %[a0]\n\tds_read_b128 " V1(0) ", %[a1]\n\t"
                 Z16(0) Z16(16)
                 "s_waitcnt lgkmcnt(0)\n\ts_barrier\n\t"
                 "v_mfma_f32_32x32x16_bf16 v[36:51], " KS(1) ", %[qa], 0\n\t"
                 "v_mfma_f32_32x32x16_bf16 v[52:67], " KS(1) ", %[qb], 0\n\t"
                 "s_nop 15\n\ts_nop 3\n\t"
                 : "=&{v[36:51]}"(sa), "=&{v[52:67]}"(sb)
                 : [lm] "v"(lm), [la] "v"(la), [a0] "v"(lv0), [a1] "v"(lv1), [qa] "v"(qa), [qb] "v"(qb)
                 : "memory", ACLOB);
    const float nm = -halves_max(fmaxf(max16(sa), max16(sb)));
#pragma unroll
    for (int i = 0; i < 16; ++i) mm[i] = nm;
    asm volatile("s_nop 1\n\t" QKA(0) QKB(0) "s_nop 11\n\t"
                 EX4(36) EX4(40) EX4(44) SMF(OA, V0(0), 36) EX4(48) EX4(52) SMF(OA, V1(0), 44) EX4(56) EX4(60) SMF(OB, V0(0), 52) EX4(64)
                 : "=&{v[36:51]}"(sa), "=&{v[52:67]}"(sb)
                 : "{v[68:83]}"(mm), [qa] "v"(qa), [qb] "v"(qb), [idx] "v"(idx) : "memory", ACLOB);
  }
  ST(4);
  ST(12);
  const unsigned kvb = WS_KV + (head * 2 + kh) * 32 * KVB;
  const unsigned tbl = kvb + ((lane + rb) & 31) * KVB;
  unsigned todo = 0xfffffffeu, ready = 0;
  int spins = 0;
  auto pick = [&]() -> unsigned {
    while (!(ready & todo)) {
      if (spins) __builtin_amdgcn_s_sleep(1);
      ready = __builtin_amdgcn_readfirstlane(LDSU(L_CTL));
      if (++spins > (SPIN_MAX << 4)) ready = ~0u;
    }
    const int i = __builtin_ctz(ready & todo);
    todo &= ~(1u << i);
    return __builtin_amdgcn_readlane(tbl, i);
  };
  unsigned s3, s4, s5, s6, s7;
  {
    const unsigned s1 = pick(), s2 = pick();
    s3 = pick(); s4 = pick();
    ST(5);
    asm volatile("s_nop 4\n\t" LDK(1, "%[s1]") LDV(1, "%[s1]") LDK(2, "%[s2]") LDV(2, "%[s2]") LDK(3, "%[s3]") LDV(3, "%[s3]") LDK(0, "%[s4]")
                 "s_waitcnt vmcnt(9)\n\t" QKA(1) "s_nop 15\n\t"
                 : "=&{v[36:51]}"(sa)
                 : "{v[68:83]}"(mm), [qa] "v"(qa), [vk] "v"(vk), [v0] "v"(vv0), [v1] "v"(vv1), [rs] "s"(rs),
                   [s1] "s"(s1), [s2] "s"(s2), [s3] "s"(s3), [s4] "s"(s4)
                 : "memory", ACLOB);
  }
  ST(6);
  __builtin_amdgcn_s_setprio(2);
  s5 = pick(); TILE(1, 0, 2, s4, s5);
  s6 = pick(); TILE(2, 1, 3, s5, s6);
  s7 = pick(); TILE(3, 2, 0, s6, s7);
  ST(20);
#pragma unroll 1
  for (int t = 4; t < 28; t += 4) {
    if (t == 8) ST(21);
    if (t == 20) __builtin_amdgcn_s_setprio(1);
    s4 = pick(); TILE(0, 3, 1, s7, s4);
    s5 = pick(); TILE(1, 0, 2, s4, s5);
    s6 = pick(); TILE(2, 1, 3, s5, s6);
    s7 = pick(); TILE(3, 2, 0, s6, s7);
  }
  ST(22);
  TILE(0, 3, 1, s7, s7);
  TILE(1, 0, 2, s7, s7);
  __builtin_amdgcn_s_setprio(0);
  TILE(2, 1, 3, s7, s7);
  TILE(3, 2, 0, s7, s7);
  float oa[9], ob[9];
#define RD(i, r) "v_accvgpr_read_b32 %" STR(i) ", a" STR(r) "\n\t"
  asm volatile("s_nop 1\n\t" SMF(OB, V1(3), 60) "s_waitcnt vmcnt(0)\n\ts_nop 15\n\ts_nop 3\n\t"
               RD(0, 0) RD(1, 1) RD(2, 2) RD(3, 3) RD(4, 4) RD(5, 5) RD(6, 6) RD(7, 7) RD(8, 8)
               RD(9, 16) RD(10, 17) RD(11, 18) RD(12, 19) RD(13, 20) RD(14, 21) RD(15, 22) RD(16, 23) RD(17, 24)
               : "=v"(oa[0]), "=v"(oa[1]), "=v"(oa[2]), "=v"(oa[3]), "=v"(oa[4]), "=v"(oa[5]), "=v"(oa[6]), "=v"(oa[7]), "=v"(oa[8]),
                 "=v"(ob[0]), "=v"(ob[1]), "=v"(ob[2]), "=v"(ob[3]), "=v"(ob[4]), "=v"(ob[5]), "=v"(ob[6]), "=v"(ob[7]), "=v"(ob[8])
               : "{v[52:67]}"(sb), [idx] "v"(idx) : "memory", ACLOB);
  ST(7);
  TAIL(ws, x, out, rs, b, w, oa, ob, qa, qb, vk, vv0, vv1, idx, gate, tlo, thi STPASS);
  STDUMP();
}

extern "C" void kernel_launch(void* const* d_in, const int* in_sizes, int n_in, void* d_out, int out_size, void* d_ws, size_t ws_size,
                              hipStream_t stream) {
  (void)hipFuncSetAttribute((const void*)fused, hipFuncAttributeMaxDynamicSharedMemorySize, LDS_BYTES);
  fused<<<256, 576, LDS_BYTES, stream>>>((const u16*)d_in[0], (const u16*)d_in[1], (const u16*)d_in[2], (const u16*)d_in[4],
                                         (const u16*)d_in[5], (const u16*)d_in[6], (const u16*)d_in[7], (const u16*)d_in[8],
                                         (char*)d_ws, (u16*)d_out);
}
```

```cpp
#include <hip/hip_runtime.h>
#include <hip/hip_bf16.h>

typedef unsigned short u16;
typedef unsigned int u32;
typedef unsigned long long u64;
typedef __attribute__((ext_vector_type(8))) __bf16 bf16x8;
typedef __attribute__((ext_vector_type(16))) __bf16 bf16x16;
typedef __attribute__((ext_vector_type(16))) float f32x16;
typedef __attribute__((ext_vector_type(8))) float f32x8;
typedef __attribute__((ext_vector_type(4))) float f32x4;
typedef __attribute__((ext_vector_type(4))) unsigned u32x4;
typedef __attribute__((ext_vector_type(2))) unsigned u32x2;
typedef __attribute__((ext_vector_type(4))) short s16x4;
typedef __amdgpu_buffer_rsrc_t rsrc_t;
#define LAS __attribute__((address_space(3)))

constexpr int D_ = 512, F_ = 1792;
constexpr u32 KVB = 2048;
constexpr u32 WS_KV = 0;
constexpr u32 WS_OF = 2048 * KVB;
constexpr u32 WS_FL = WS_OF + (2u << 20);
constexpr u32 WS_CL = WS_FL + 131072;
constexpr u32 WS_P1 = WS_CL + 32768;
constexpr u32 WS_F1 = WS_P1 + 114688;
constexpr u32 WS_P2 = WS_F1 + 28672;
constexpr u32 WS_F2 = WS_P2 + 57344;
constexpr u32 WS_EP = WS_F2 + 28672;
constexpr u32 WS_END = WS_EP + 128;
constexpr u32 L_X = 0, L_W = 65536, L_KV = 98304, L_WV = 147456, L_QX = 149504;
constexpr u32 L_G = 0, L_U = 8192, L_WD = 16384, L_XS = 24576, L_HS = 25600;
constexpr u32 L_OS = 131072;
constexpr u32 L_SY = 158208;
constexpr u32 L_RG = 163552;
constexpr u32 L_CTL = 163808;
constexpr int LDS_BYTES = 163840;
constexpr int SPIN_MAX = 1 << 18;
constexpr int CODE_KB = 24;
#define TSTEP 11
#define TFIRST 2
#define CFIRST 5
#define WSLEEP 12
constexpr int PRIO1 = 22, PRIO0 = 29;

__device__ __forceinline__ rsrc_t mkrs(const void* p, unsigned bytes) { return __builtin_amdgcn_make_buffer_rsrc((void*)p, 0, (int)bytes, 0x00020000); }
template <unsigned ADD>
__device__ __forceinline__ void dma(unsigned voff, rsrc_t r, unsigned soff, unsigned m) {
  asm volatile("s_add_u32 m0, %0, %c4\n\ts_nop 0\n\tbuffer_load_dwordx4 %1, %2, %3 offen lds" :: "s"(m), "v"(voff), "s"(r), "s"(soff), "i"(ADD) : "memory", "scc");
}
__device__ __forceinline__ void touch(unsigned voff, rsrc_t r) {
  asm volatile("buffer_load_dword v83, %0, %1, 0 offen" :: "v"(voff), "s"(r) : "memory", "v83");
}
__device__ __forceinline__ unsigned cvtpk(float lo, float hi) {
  typedef __attribute__((ext_vector_type(2))) __bf16 bf16x2;
  typedef __attribute__((ext_vector_type(2))) float f32x2;
  f32x2 v = {lo, hi};
  return __builtin_bit_cast(unsigned, __builtin_convertvector(v, bf16x2));
}
__device__ __forceinline__ float bfround(float f) { return __uint_as_float(cvtpk(f, 0.f) << 16); }
__device__ __forceinline__ float bflo(unsigned u) { return __uint_as_float(u << 16); }
__device__ __forceinline__ float bfhi(unsigned u) { return __uint_as_float(u & 0xffff0000u); }
__device__ __forceinline__ float sigmoidf_(float z) { return __builtin_amdgcn_rcpf(1.f + __expf(-z)); }
__device__ __forceinline__ bf16x8 lds_b128(unsigned a) { return *(const LAS bf16x8*)(size_t)a; }
__device__ __forceinline__ void lds_w128(unsigned a, u32x4 v) { *(LAS u32x4*)(size_t)a = v; }
__device__ __forceinline__ s16x4 lds_tr(unsigned a) { return __builtin_amdgcn_ds_read_tr16_b64_v4i16((LAS s16x4*)(size_t)a); }
__device__ __forceinline__ bf16x8 cat8(s16x4 a, s16x4 b) {
  typedef __attribute__((ext_vector_type(8))) short s16x8;
  s16x8 r = {a[0], a[1], a[2], a[3], b[0], b[1], b[2], b[3]};
  return __builtin_bit_cast(bf16x8, r);
}
__device__ __forceinline__ u32x4 pack8(const f32x16& a, int z, float s) {
  return u32x4{cvtpk(a[8 * z] * s, a[8 * z + 1] * s), cvtpk(a[8 * z + 2] * s, a[8 * z + 3] * s),
               cvtpk(a[8 * z + 4] * s, a[8 * z + 5] * s), cvtpk(a[8 * z + 6] * s, a[8 * z + 7] * s)};
}
__device__ __forceinline__ u32x4 pack8(const f32x16& a, int z) {
  return u32x4{cvtpk(a[8 * z], a[8 * z + 1]), cvtpk(a[8 * z + 2], a[8 * z + 3]), cvtpk(a[8 * z + 4], a[8 * z + 5]), cvtpk(a[8 * z + 6], a[8 * z + 7])};
}
__device__ __forceinline__ float max16(const f32x16& p) {
  float a = fmaxf(fmaxf(p[0], p[1]), p[2]), b = fmaxf(fmaxf(p[3], p[4]), p[5]);
  a = fmaxf(fmaxf(a, p[6]), p[7]); b = fmaxf(fmaxf(b, p[8]), p[9]);
  a = fmaxf(fmaxf(a, p[10]), p[11]); b = fmaxf(fmaxf(b, p[12]), p[13]);
  a = fmaxf(fmaxf(a, p[14]), p[15]);
  return fmaxf(a, b);
}
__device__ __forceinline__ float halves_max(float m) {
  auto rr = __builtin_amdgcn_permlane32_swap(__float_as_uint(m), __float_as_uint(m), false, false);
  return fmaxf(__uint_as_float(rr[0]), __uint_as_float(rr[1]));
}
__device__ __forceinline__ int fresh_lane() { int l; asm volatile("v_mbcnt_lo_u32_b32 %0, -1, 0\n\tv_mbcnt_hi_u32_b32 %0, -1, %0" : "=v"(l)); return l; }
#define MFMA32(a, b, c) __builtin_amdgcn_mfma_f32_32x32x16_bf16(a, b, c, 0, 0, 0)
#define MFMA16(a, b, c) __builtin_amdgcn_mfma_f32_16x16x32_bf16(a, b, c, 0, 0, 0)
#define WAIT_BAR(N) asm volatile("s_waitcnt vmcnt(" #N ") lgkmcnt(0)\n\ts_barrier" ::: "memory")
#define VM_BAR(N) asm volatile("s_waitcnt vmcnt(" #N ")\n\ts_barrier" ::: "memory")
#define BAR() asm volatile("s_waitcnt lgkmcnt(0)\n\ts_barrier" ::: "memory")
#define DRAIN() asm volatile("s_waitcnt vmcnt(0)" ::: "memory")
#define SBAR() __builtin_amdgcn_sched_barrier(0)
#define FRESH() asm volatile("" ::: "memory")
#define LDSU(a) (*(volatile LAS u32*)(size_t)(a))
#define LD128(off) __builtin_amdgcn_raw_buffer_load_b128(rs, (off), 0, 16)
#define LD64(off) __builtin_amdgcn_raw_buffer_load_b64(rs, (off), 0, 16)
#define ST128(v, off) __builtin_amdgcn_raw_buffer_store_b128((v), rs, (off), 0, 16)
#define ST64(v, off) __builtin_amdgcn_raw_buffer_store_b64((v), rs, (off), 0, 16)
#define GET_TAG() u64 ep_; asm volatile("s_load_dwordx2 %0, %1, 0x0\n\ts_waitcnt lgkmcnt(0)" : "=s"(ep_) : "s"(ws + WS_EP) : "memory"); \
  const u64 tag_ = ep_ * 0x9E3779B97F4A7C15ull + 0xD1B54A32D192ED03ull; const u32 thi = (u32)(tag_ >> 32), tlo = (u32)tag_

#define STR_(x) #x
#define STR(x) STR_(x)
#define EX1(b) "v_exp_f32 v[" STR(b) "], v[" STR(b) "]\n\t"
#define EX4(b) EX1(b) EX1(b+1) EX1(b+2) EX1(b+3)
#define AR4(b) "a[" STR(b) ":" STR(b) "+3]"
#define KS(s) AR4(32+4*s)
#define V0(s) AR4(48+8*s)
#define V1(s) AR4(52+8*s)
#define SMF(acc, va, sb) "v_smfmac_f32_32x32x32_bf16 " acc ", " va ", v[" STR(sb) ":" STR(sb) "+7], %[idx]\n\t"
#define OA "a[0:15]"
#define OB "a[16:31]"
#define QKA(s) "v_mfma_f32_32x32x16_bf16 v[36:51], " KS(s) ", %[qa], 0\n\t"
#define QKB(s) "v_mfma_f32_32x32x16_bf16 v[52:67], " KS(s) ", %[qb], 0\n\t"
#define LDK(s, so) "buffer_load_dwordx4 " KS(s) ", %[vk], %[rs], " so " offen sc1\n\t"
#define LDV(s, so) "s_mov_b64 exec, %[lm]\n\tbuffer_load_dwordx4 " V0(s) ", %[v0], %[rs], " so " offen offset:1024 sc1\n\t" \
                   "buffer_load_dwordx4 " V1(s) ", %[v0], %[rs], " so " offen offset:1536 sc1\n\ts_mov_b64 exec, -1\n\t"
#define ACLOB "a0","a1","a2","a3","a4","a5","a6","a7","a8","a9","a10","a11","a12","a13","a14","a15","a16","a17","a18","a19", \
  "a20","a21","a22","a23","a24","a25","a26","a27","a28","a29","a30","a31","a32","a33","a34","a35","a36","a37","a38","a39", \
  "a40","a41","a42","a43","a44","a45","a46","a47","a48","a49","a50","a51","a52","a53","a54","a55","a56","a57","a58","a59", \
  "a60","a61","a62","a63","a64","a65","a66","a67","a68","a69","a70","a71","a72","a73","a74","a75","a76","a77","a78","a79"
#define TILE(s, sp, sn, soV, soK) \
  asm volatile(EX4(36) SMF(OB, V1(sp), 60) LDV(sp, "%[sv]") EX4(40) QKB(s) LDK(s, "%[sk]") EX4(44) \
               "s_waitcnt vmcnt(9)\n\t" SMF(OA, V0(s), 36) EX4(48) \
               EX4(52) SMF(OA, V1(s), 44) EX4(56) QKA(sn) EX4(60) SMF(OB, V0(s), 52) EX4(64) \
               : "+{v[36:51]}"(sa), "+{v[52:67]}"(sb) \
               : [qa] "v"(qa), [qb] "v"(qb), [idx] "v"(idx), [vk] "v"(vk), [v0] "v"(vv0), [lm] "s"(lm), [rs] "s"(rs), \
                 [sv] "s"(soV), [sk] "s"(soK) : "memory", ACLOB)
#define OP_PRE \
  "ds_read_b64_tr_b16 a[0:1], %0 offset:0\n\tds_read_b64_tr_b16 a[2:3], %0 offset:8192\n\t" \
  "ds_read_b64_tr_b16 a[4:5], %0 offset:16384\n\tds_read_b64_tr_b16 a[6:7], %0 offset:24576\n\t" \
  "ds_read_b64_tr_b16 a[8:9], %0 offset:32768\n\tds_read_b64_tr_b16 a[10:11], %0 offset:40960\n\t" \
  "ds_read_b64_tr_b16 a[12:13], %0 offset:49152\n\tds_read_b64_tr_b16 a[14:15], %0 offset:57344\n\t" \
  "ds_read_b64_tr_b16 a[16:17], %1 offset:0\n\tds_read_b64_tr_b16 a[18:19], %1 offset:8192\n\t" \
  "ds_read_b64_tr_b16 a[20:21], %1 offset:16384\n\tds_read_b64_tr_b16 a[22:23], %1 offset:24576\n\t" \
  "ds_read_b64_tr_b16 a[24:25], %1 offset:32768\n\tds_read_b64_tr_b16 a[26:27], %1 offset:40960\n\t" \
  "ds_read_b64_tr_b16 a[28:29], %1 offset:49152\n\tds_read_b64_tr_b16 a[30:31], %1 offset:57344\n\t" \
  "ds_read_b64_tr_b16 a[32:33], %0 offset:256\n\tds_read_b64_tr_b16 a[34:35], %0 offset:8448\n\t" \
  "ds_read_b64_tr_b16 a[36:37], %0 offset:16640\n\tds_read_b64_tr_b16 a[38:39], %0 offset:24832\n\t" \
  "ds_read_b64_tr_b16 a[40:41], %0 offset:33024\n\tds_read_b64_tr_b16 a[42:43], %0 offset:41216\n\t" \
  "ds_read_b64_tr_b16 a[44:45], %0 offset:49408\n\tds_read_b64_tr_b16 a[46:47], %0 offset:57600\n\t" \
  "ds_read_b64_tr_b16 a[48:49], %1 offset:256\n\tds_read_b64_tr_b16 a[50:51], %1 offset:8448\n\t" \
  "ds_read_b64_tr_b16 a[52:53], %1 offset:16640\n\tds_read_b64_tr_b16 a[54:55], %1 offset:24832\n\t" \
  "ds_read_b64_tr_b16 a[56:57], %1 offset:33024\n\tds_read_b64_tr_b16 a[58:59], %1 offset:41216\n\t" \
  "ds_read_b64_tr_b16 a[60:61], %1 offset:49408\n\tds_read_b64_tr_b16 a[62:63], %1 offset:57600\n\t" \
  "ds_read_b64_tr_b16 a[64:65], %0 offset:512\n\tds_read_b64_tr_b16 a[66:67], %0 offset:8704\n\t" \
  "ds_read_b64_tr_b16 a[68:69], %0 offset:16896\n\tds_read_b64_tr_b16 a[70:71], %0 offset:25088\n\t" \
  "ds_read_b64_tr_b16 a[72:73], %0 offset:33280\n\tds_read_b64_tr_b16 a[74:75], %0 offset:41472\n\t" \
  "ds_read_b64_tr_b16 a[76:77], %0 offset:49664\n\tds_read_b64_tr_b16 a[78:79], %0 offset:57856\n\t" \
  ""
#define OP_MAIN \
  "ds_read_b128 v[20:23], %[b] offset:0\n\tds_read_b128 v[24:27], %[b] offset:1024\n\t" \
  "ds_read_b128 v[28:31], %[b] offset:2048\n\tds_read_b128 v[32:35], %[b] offset:3072\n\t" \
  "ds_read_b128 v[36:39], %[b] offset:4096\n\tds_read_b128 v[40:43], %[b] offset:5120\n\t" \
  "ds_read_b128 v[44:47], %[b] offset:6144\n\tds_read_b128 v[48:51], %[b] offset:7168\n\t" \
  "s_waitcnt lgkmcnt(7)\n\tv_mfma_f32_32x32x16_bf16 v[68:83], a[0:3], v[20:23], 0\n\t" \
  "ds_read_b128 v[20:23], %[b] offset:8192\n\ts_waitcnt lgkmcnt(7)\n\t" \
  "v_mfma_f32_32x32x16_bf16 v[68:83], a[4:7], v[24:27], v[68:83]\n\tds_read_b128 v[24:27], %[b] offset:9216\n\t" \
  "s_waitcnt lgkmcnt(7)\n\tv_mfma_f32_32x32x16_bf16 v[68:83], a[8:11], v[28:31], v[68:83]\n\t" \
  "ds_read_b128 v[28:31], %[b] offset:10240\n\ts_waitcnt lgkmcnt(7)\n\t" \
  "v_mfma_f32_32x32x16_bf16 v[68:83], a[12:15], v[32:35], v[68:83]\n\tds_read_b128 v[32:35], %[b] offset:11264\n\t" \
  "s_waitcnt lgkmcnt(7)\n\tv_mfma_f32_32x32x16_bf16 v[68:83], a[16:19], v[36:39], v[68:83]\n\t" \
  "ds_read_b128 v[36:39], %[b] offset:12288\n\tds_read_b64_tr_b16 a[0:1], %[a1] offset:512\n\t" \
  "ds_read_b64_tr_b16 a[2:3], %[a1] offset:8704\n\ts_waitcnt lgkmcnt(9)\n\t" \
  "v_mfma_f32_32x32x16_bf16 v[68:83], a[20:23], v[40:43], v[68:83]\n\tds_read_b128 v[40:43], %[b] offset:13312\n\t" \
  "ds_read_b64_tr_b16 a[4:5], %[a1] offset:16896\n\tds_read_b64_tr_b16 a[6:7], %[a1] offset:25088\n\t" \
  "s_waitcnt lgkmcnt(11)\n\tv_mfma_f32_32x32x16_bf16 v[68:83], a[24:27], v[44:47], v[68:83]\n\t" \
  "ds_read_b128 v[44:47], %[b] offset:14336\n\tds_read_b64_tr_b16 a[8:9], %[a1] offset:33280\n\t" \
  "ds_read_b64_tr_b16 a[10:11], %[a1] offset:41472\n\ts_waitcnt lgkmcnt(13)\n\t" \
  "v_mfma_f32_32x32x16_bf16 v[68:83], a[28:31], v[48:51], v[68:83]\n\tds_read_b128 v[48:51], %[b] offset:15360\n\t" \
  "ds_read_b64_tr_b16 a[12:13], %[a1] offset:49664\n\tds_read_b64_tr_b16 a[14:15], %[a1] offset:57856\n\t" \
  "s_waitcnt lgkmcnt(15)\n\tv_mfma_f32_32x32x16_bf16 v[68:83], a[32:35], v[20:23], v[68:83]\n\t" \
  "ds_read_b128 v[20:23], %[b] offset:16384\n\tds_read_b64_tr_b16 a[16:17], %[a0] offset:768\n\t" \
  "ds_read_b64_tr_b16 a[18:19], %[a0] offset:8960\n\ts_waitcnt lgkmcnt(15)\n\t" \
  "v_mfma_f32_32x32x16_bf16 v[68:83], a[36:39], v[24:27], v[68:83]\n\tds_read_b128 v[24:27], %[b] offset:17408\n\t" \
  "ds_read_b64_tr_b16 a[20:21], %[a0] offset:17152\n\tds_read_b64_tr_b16 a[22:23], %[a0] offset:25344\n\t" \
  "s_waitcnt lgkmcnt(15)\n\tv_mfma_f32_32x32x16_bf16 v[68:83], a[40:43], v[28:31], v[68:83]\n\t" \
  "ds_read_b128 v[28:31], %[b] offset:18432\n\tds_read_b64_tr_b16 a[24:25], %[a0] offset:33536\n\t" \
  "ds_read_b64_tr_b16 a[26:27], %[a0] offset:41728\n\ts_waitcnt lgkmcnt(15)\n\t" \
  "v_mfma_f32_32x32x16_bf16 v[68:83], a[44:47], v[32:35], v[68:83]\n\tds_read_b128 v[32:35], %[b] offset:19456\n\t" \
  "ds_read_b64_tr_b16 a[28:29], %[a0] offset:49920\n\tds_read_b64_tr_b16 a[30:31], %[a0] offset:58112\n\t" \
  "s_waitcnt lgkmcnt(15)\n\tv_mfma_f32_32x32x16_bf16 v[68:83], a[48:51], v[36:39], v[68:83]\n\t" \
  "ds_read_b128 v[36:39], %[b] offset:20480\n\tds_read_b64_tr_b16 a[32:33], %[a1] offset:768\n\t" \
  "ds_read_b64_tr_b16 a[34:35], %[a1] offset:8960\n\ts_waitcnt lgkmcnt(15)\n\t" \
  "v_mfma_f32_32x32x16_bf16 v[68:83], a[52:55], v[40:43], v[68:83]\n\tds_read_b128 v[40:43], %[b] offset:21504\n\t" \
  "ds_read_b64_tr_b16 a[36:37], %[a1] offset:17152\n\tds_read_b64_tr_b16 a[38:39], %[a1] offset:25344\n\t" \
  "s_waitcnt lgkmcnt(15)\n\tv_mfma_f32_32x32x16_bf16 v[68:83], a[56:59], v[44:47], v[68:83]\n\t" \
  "ds_read_b128 v[44:47], %[b] offset:22528\n\tds_read_b64_tr_b16 a[40:41], %[a1] offset:33536\n\t" \
  "ds_read_b64_tr_b16 a[42:43], %[a1] offset:41728\n\ts_waitcnt lgkmcnt(15)\n\t" \
  "v_mfma_f32_32x32x16_bf16 v[68:83], a[60:63], v[48:51], v[68:83]\n\tds_read_b128 v[48:51], %[b] offset:23552\n\t" \
  "ds_read_b64_tr_b16 a[44:45], %[a1] offset:49920\n\tds_read_b64_tr_b16 a[46:47], %[a1] offset:58112\n\t" \
  "s_waitcnt lgkmcnt(15)\n\tv_mfma_f32_32x32x16_bf16 v[68:83], a[64:67], v[20:23], v[68:83]\n\t" \
  "ds_read_b128 v[20:23], %[b] offset:24576\n\ts_waitcnt lgkmcnt(15)\n\t" \
  "v_mfma_f32_32x32x16_bf16 v[68:83], a[68:71], v[24:27], v[68:83]\n\tds_read_b128 v[24:27], %[b] offset:25600\n\t" \
  "s_waitcnt lgkmcnt(15)\n\tv_mfma_f32_32x32x16_bf16 v[68:83], a[72:75], v[28:31], v[68:83]\n\t" \
  "ds_read_b128 v[28:31], %[b] offset:26624\n\ts_waitcnt lgkmcnt(15)\n\t" \
  "v_mfma_f32_32x32x16_bf16 v[68:83], a[76:79], v[32:35], v[68:83]\n\tds_read_b128 v[32:35], %[b] offset:27648\n\t" \
  "s_waitcnt lgkmcnt(15)\n\tv_mfma_f32_32x32x16_bf16 v[68:83], a[0:3], v[36:39], v[68:83]\n\t" \
  "ds_read_b128 v[36:39], %[b] offset:28672\n\ts_waitcnt lgkmcnt(13)\n\t" \
  "v_mfma_f32_32x32x16_bf16 v[68:83], a[4:7], v[40:43], v[68:83]\n\tds_read_b128 v[40:43], %[b] offset:29696\n\t" \
  "s_waitcnt lgkmcnt(11)\n\tv_mfma_f32_32x32x16_bf16 v[68:83], a[8:11], v[44:47], v[68:83]\n\t" \
  "ds_read_b128 v[44:47], %[b] offset:30720\n\ts_waitcnt lgkmcnt(9)\n\t" \
  "v_mfma_f32_32x32x16_bf16 v[68:83], a[12:15], v[48:51], v[68:83]\n\tds_read_b128 v[48:51], %[b] offset:31744\n\t" \
  "s_waitcnt lgkmcnt(7)\n\tv_mfma_f32_32x32x16_bf16 v[68:83], a[16:19], v[20:23], v[68:83]\n\t" \
  "s_waitcnt lgkmcnt(6)\n\tv_mfma_f32_32x32x16_bf16 v[68:83], a[20:23], v[24:27], v[68:83]\n\t" \
  "s_waitcnt lgkmcnt(5)\n\tv_mfma_f32_32x32x16_bf16 v[68:83], a[24:27], v[28:31], v[68:83]\n\t" \
  "s_waitcnt lgkmcnt(4)\n\tv_mfma_f32_32x32x16_bf16 v[68:83], a[28:31], v[32:35], v[68:83]\n\t" \
  "s_waitcnt lgkmcnt(3)\n\tv_mfma_f32_32x32x16_bf16 v[68:83], a[32:35], v[36:39], v[68:83]\n\t" \
  "s_waitcnt lgkmcnt(2)\n\tv_mfma_f32_32x32x16_bf16 v[68:83], a[36:39], v[40:43], v[68:83]\n\t" \
  "s_waitcnt lgkmcnt(1)\n\tv_mfma_f32_32x32x16_bf16 v[68:83], a[40:43], v[44:47], v[68:83]\n\t" \
  "s_waitcnt lgkmcnt(0)\n\tv_mfma_f32_32x32x16_bf16 v[68:83], a[44:47], v[48:51], v[68:83]\n\t" \
  "s_nop 15\n\t" \
  ""

__global__ void __launch_bounds__(768, 3)
fused(const u16* __restrict__ x, const u16* __restrict__ W, const u16* __restrict__ wo, const u16* __restrict__ wr,
      const u16* __restrict__ wg, const u16* __restrict__ wu, const u16* __restrict__ wd, const u16* __restrict__ gw,
      char* __restrict__ ws, u16* __restrict__ out) {
  u64 pc0; asm volatile("s_getpc_b64 %0" : "=s"(pc0));
  const int b = blockIdx.x;
  const int hg = b & 7, rb = b >> 3;
  const int w = __builtin_amdgcn_readfirstlane(threadIdx.x >> 6);
  const int tt = 2 * rb + (hg >> 2), cg = hg & 3;
  const rsrc_t rs = mkrs(ws, WS_END);

  asm volatile("" :: "s"(x), "s"(W), "s"(wr), "s"(gw), "s"(ws));
  const int h = w & 3, rot = (rb + hg) & 7;
  const rsrc_t rx = mkrs(x, 2048 * D_ * 2), rw = mkrs(W, D_ * 1536 * 2);
#define LOADER_ADDR(l_) const unsigned vx0 = (rb * 64 + h * 16 + ((l_) >> 3)) * 1024 + ((((l_) & 7) ^ ((l_) >> 4)) << 4), vx1 = (vx0 + 8192) ^ 64; \
  const unsigned vw = ((l_) >> 3) * 3072 + hg * 128 + ((((l_) & 7) ^ ((((l_) >> 4) & 1) << 2)) << 4), vwk = vw + h * (8 * 3072) + 1024, vwv = vwk + 1024
  {
    LOADER_ADDR(threadIdx.x & 63);
    const unsigned g0 = w < 8 ? 0 : 2;
    unsigned mx = L_X + h * 2048 + g0 * 8192, mw = L_W + h * 1024 + g0 * 16384, kb = (rot + g0) & 7, sx, sw;
    asm volatile("s_cmp_lt_u32 %[w], 4\n\ts_cbranch_scc1 .Lfirst_done%=\n\ts_setprio 3\n\ts_cmp_lt_u32 %[w], 8\n\ts_cbranch_scc1 .Lfirst%=\n\ts_sleep 6\n"
                 ".Lfirst%=:\n\t"
                 "s_mov_b32 m0, %[mx]\n\ts_lshl_b32 %[sx], %[kb], 7\n\tbuffer_load_dwordx4 %[x0], %[rx], %[sx] offen lds\n\t"
                 "s_add_u32 m0, %[mx], 1024\n\ts_mul_i32 %[sw], %[kb], 196608\n\tbuffer_load_dwordx4 %[x1], %[rx], %[sx] offen lds\n\t"
                 "s_mov_b32 m0, %[mw]\n\ts_add_u32 %[kb], %[kb], 1\n\tbuffer_load_dwordx4 %[wk], %[rw], %[sw] offen lds\n\t"
                 "s_add_u32 m0, %[mw], 4096\n\ts_add_u32 %[sx], %[sw], 98304\n\tbuffer_load_dwordx4 %[wv], %[rw], %[sw] offen lds\n\t"
                 "s_add_u32 m0, %[mw], 8192\n\ts_and_b32 %[kb], %[kb], 7\n\tbuffer_load_dwordx4 %[wk], %[rw], %[sx] offen lds\n\t"
                 "s_add_u32 m0, %[mw], 12288\n\ts_add_u32 %[mx], %[mx], 8192\n\tbuffer_load_dwordx4 %[wv], %[rw], %[sx] offen lds\n\t"
                 "s_add_u32 %[mw], %[mw], 16384\n\ts_cmp_lt_u32 %[mx], %[me]\n\ts_cbranch_scc1 .Lfirst%=\n"
                 ".Lfirst_done%=:"
                 : [mx] "+s"(mx), [mw] "+s"(mw), [kb] "+s"(kb), [sx] "=&s"(sx), [sw] "=&s"(sw)
                 : [w] "s"(w), [me] "s"(w < 8 ? 16384u : 40960u), [x0] "v"(vx0), [x1] "v"(vx1), [wk] "v"(vwk), [wv] "v"(vwv), [rx] "s"(rx), [rw] "s"(rw) : "memory", "scc");
  }
  if (w == 0 && (b >> 3) < CODE_KB)
    asm volatile("global_load_dword v83, %0, off" :: "v"((const char*)(pc0 & ~0xffull) + (b >> 3) * 1024 + (threadIdx.x << 4)) : "memory", "v83");
  if (__builtin_expect(w >= 8, 1)) {
    const int lane = fresh_lane(), q4 = (lane & 15) >> 2, p4 = lane & 3;
    const int xj = b & 7, xc = b >> 3;
#define KBLK(g) ((unsigned)(((g) + rot) & 7))
#define QSL(r) ((((r) + 6) % 10) * 8192)
#define ISSQ(r) do { const unsigned so_ = KBLK(r) * 196608; dma<L_W + QSL(r)>(vq0, rw, so_, m2); dma<L_W + QSL(r) + 1024>(vq1, rw, so_, m2); } while (0)
    LOADER_ADDR(lane);
    const unsigned vq0 = vw + h * (16 * 3072), vq1 = vq0 + 8 * 3072, m1 = h * 1024, m2 = h * 2048;
#define WSL(r) (L_W + ((r) % 10) * 8192)
#define ISSX(g) do { const unsigned so_ = KBLK(g) * 128; dma<L_X + (g) * 8192>(vx0, rx, so_, m2); dma<L_X + (g) * 8192 + 1024>(vx1, rx, so_, m2); } while (0)
#define ISSW(g) do { const unsigned so_ = KBLK(g) * 196608; dma<WSL(2 * (g))>(vwk, rw, so_, m1); dma<WSL(2 * (g)) + 4096>(vwv, rw, so_, m1); \
      dma<WSL(2 * (g) + 1)>(vwk, rw, so_ + 98304, m1); dma<WSL(2 * (g) + 1) + 4096>(vwv, rw, so_ + 98304, m1); } while (0)
    if (w == 8) { dma<L_WV>(((lane + 8 * rot) & 63) * 16, mkrs(wr, 1024), 0, 0); if (lane < 8) LDSU(L_CTL + lane * 4) = 0; }
    if (w == 11) dma<L_WV + 1024>(((lane + 8 * rot) & 63) * 16, mkrs(gw, 1024), 0, 0);
    GET_TAG();
    const rsrc_t ro = mkrs(wo, D_ * D_ * 2);
    asm volatile("" :: "s"(ro), "s"(tlo), "s"(thi));
    WAIT_BAR(12); ISSX(5); ISSW(5); ISSX(6);
    VM_BAR(14); ISSW(6); ISSX(7);
    VM_BAR(14); ISSW(7);
    VM_BAR(12); ISSQ(0); ISSQ(1); ISSQ(2);
    VM_BAR(12); ISSQ(3); ISSQ(4); ISSQ(5);
    VM_BAR(12); ISSQ(6); ISSQ(7);
    asm volatile("s_barrier" ::: "memory");
    VM_BAR(8);
    VM_BAR(0);
    const unsigned flo = WS_FL + ((hg * 4 + h) * 32 + ((lane + rb) & 31)) * 128;
    asm volatile("s_barrier\n\ts_barrier" ::: "memory");
    const unsigned vb = (lane >> 4) * 131072 + cg * 256 + (lane & 15) * 16;
#define WOG(g, SLP) asm volatile("s_mov_b32 m0, %0\n\ts_sleep " STR(SLP) "\n\t" \
                     "buffer_load_dwordx4 %2, %1, %0 offen lds\n\tbuffer_load_dwordx4 %3, %1, %0 offen offset:1024 lds\n\t" \
                     "buffer_load_dwordx4 %4, %1, %0 offen offset:2048 lds\n\tbuffer_load_dwordx4 %5, %1, %0 offen offset:3072 lds" \
                     :: "s"((g) * 4096), "s"(ro), "v"(vb), "v"(vb ^ 64), "v"(vb ^ 128), "v"(vb ^ 192) : "memory")
#define WORANGE(g0, n, SLP) do { int g_ = (xc >> 1) % (n); _Pragma("unroll 1") for (int i_ = 0; i_ < (n); ++i_) { WOG((g0) + g_, SLP); g_ = g_ + 1 == (n) ? 0 : g_ + 1; } } while (0)
    {
      unsigned key = 0;
      for (int spin = 0; spin < SPIN_MAX; ++spin) {
        if (spin) __builtin_amdgcn_s_sleep(1);
        FRESH();
        const u32x2 f = LD64(flo);
        key = f[0] & 0x7ffffffu;
        const unsigned rdy = (unsigned)__ballot(f[1] == thi && (f[0] ^ tlo) < 0x8000000u);
        LDSU(L_CTL + (w - 8) * 4) = rdy;
        if (rdy == ~0u) break;
      }
      LDSU(L_CTL + (w - 8) * 4) = ~0u;
      asm volatile("ds_max_u32 %0, %1\n\ts_waitcnt lgkmcnt(0)" :: "v"(L_CTL + 16), "v"(key) : "memory");
      if (lane == 0) __hip_atomic_fetch_add((LAS u32*)(size_t)(L_CTL + 20), 1u, __ATOMIC_RELAXED, __HIP_MEMORY_SCOPE_WORKGROUP);
    }
#define NAP(n) for (int i_ = 0; i_ < (n); ++i_) __builtin_amdgcn_s_sleep(TSTEP)
#define BEST() ({ for (int spin = 0; spin < (SPIN_MAX << 2) && __builtin_amdgcn_readfirstlane(LDSU(L_CTL + 20)) != 4; ++spin) __builtin_amdgcn_s_sleep(4); \
                  (unsigned)__builtin_amdgcn_readfirstlane(LDSU(L_CTL + 16)); })
#define WAITF(off, SLP) for (int spin = 0; spin < SPIN_MAX; ++spin) { FRESH(); const u32x2 f_ = LD64(off); if (__all(f_[0] == tlo && f_[1] == thi)) break; __builtin_amdgcn_s_sleep(SLP); }
#define PICK4(d, o) ((lane & 32) ? ((lane & 16) ? d[(o) + 3][0] : d[(o) + 2][0]) : ((lane & 16) ? d[(o) + 1][0] : d[o][0]))
    if (w == 9) {
      if (xc < 28) {
        const rsrc_t rg = mkrs(wg, D_ * F_ * 2), ru = mkrs(wu, D_ * F_ * 2), rd = mkrs(wd, D_ * F_ * 2);
        const unsigned vg = (xj * 64 + (lane >> 3)) * (F_ * 2) + xc * 128 + (lane & 7) * 16, vd = (xc * 64 + (lane >> 3)) * (D_ * 2) + xj * 128 + (lane & 7) * 16;
        NAP(TFIRST + (xc & 7));
        dma<L_XS>(lane * 16, mkrs(x, 2048 * D_ * 2), (2047 - (BEST() & 2047u)) * (D_ * 2), 0);
#pragma unroll 1
        for (int i = 0; i < 8; ++i) { dma<L_G>(vg, rg, i * (8 * F_ * 2), i * 1024); dma<L_U>(vg, ru, i * (8 * F_ * 2), i * 1024); dma<L_WD>(vd, rd, i * (8 * D_ * 2), i * 1024); }
        DRAIN();
        const unsigned ba = (8 * (lane >> 4) + q4) * 128 + p4 * 8;
        {
          f32x4 d[8];
          const bf16x8 a0 = lds_b128(L_XS + xj * 128 + (lane >> 4) * 16), a1 = lds_b128(L_XS + xj * 128 + 64 + (lane >> 4) * 16);
#pragma unroll
          for (int n = 0; n < 8; ++n) {
            const unsigned o = ba + (n >> 2) * 8192 + (n & 3) * 32;
            d[n] = MFMA16(a1, cat8(lds_tr(o + 4096), lds_tr(o + 4096 + 512)), MFMA16(a0, cat8(lds_tr(o), lds_tr(o + 512)), (f32x4{0})));
          }
          ST64((u32x2{__float_as_uint(PICK4(d, 0)), __float_as_uint(PICK4(d, 4))}), WS_P1 + (xc * 8 + xj) * 512 + lane * 8);
          DRAIN();
          if (lane < 8) ST128((u32x4{tlo, thi, tlo, thi}), WS_F1 + (xc * 8 + xj) * 128 + lane * 16);
        }
        NAP(8 - (xc & 7));
        WAITF(WS_F1 + (xc * 8 + (lane & 7)) * 128, 8);
        {
          u32x2 p[8];
#pragma unroll
          for (int i = 0; i < 8; ++i) p[i] = LD64(WS_P1 + (xc * 8 + i) * 512 + lane * 8);
          float gp = 0.f, up = 0.f;
#pragma unroll
          for (int i = 0; i < 8; ++i) { gp += __uint_as_float(p[i][0]); up += __uint_as_float(p[i][1]); }
          gp = bfround(gp);
          *(LAS u16*)(size_t)(L_HS + lane * 2) = (u16)cvtpk(bfround(gp * sigmoidf_(gp)) * bfround(up), 0.f);
          f32x4 d[4];
          const bf16x8 a0 = lds_b128(L_HS + (lane >> 4) * 16), a1 = lds_b128(L_HS + 64 + (lane >> 4) * 16);
#pragma unroll
          for (int n = 0; n < 4; ++n) {
            const unsigned o = L_WD + ba + n * 32;
            d[n] = MFMA16(a1, cat8(lds_tr(o + 4096), lds_tr(o + 4096 + 512)), MFMA16(a0, cat8(lds_tr(o), lds_tr(o + 512)), (f32x4{0})));
          }
          __builtin_amdgcn_raw_buffer_store_b32(__float_as_uint(PICK4(d, 0)), rs, WS_P2 + xc * 2048 + xj * 256 + lane * 4, 0, 16);
          DRAIN();
          if (lane < 8) ST128((u32x4{tlo, thi, tlo, thi}), WS_F2 + (xc * 8 + xj) * 128 + lane * 16);
        }
      } else {
        NAP(TFIRST + 10);
      }
      WORANGE(0, 7, WSLEEP);
    } else if (w == 8) {
      NAP(TFIRST + (xc & 7));
      touch((xc * 16 + ((lane >> 1) & 15)) * 1024 + cg * 256 + (lane & 1) * 128, ro);
      NAP(9 - (xc & 7));
      WORANGE(7, 7, WSLEEP);
      WAITF(WS_CL + (rb * 8 + (lane & 7)) * 128, 16);
      const unsigned best = BEST();
      if (((2047 - (best & 2047u)) >> 5) == (unsigned)tt) {
        WAITF(WS_F2 + ((lane < 56 ? lane >> 1 : 0) * 8 + 2 * cg + (lane & 1)) * 128, 8);
        u32x2 p[28];
#pragma unroll
        for (int i = 0; i < 28; ++i) p[i] = LD64(WS_P2 + i * 2048 + cg * 512 + lane * 8);
        SBAR();
        float y0 = 0.f, y1 = 0.f;
#pragma unroll
        for (int i = 0; i < 28; ++i) { y0 += __uint_as_float(p[i][0]); y1 += __uint_as_float(p[i][1]); }
        const unsigned srt = (best >> 11) & 0xffffu;
        const float rsel = bfround(sigmoidf_(bflo((srt & 0x8000u) ? (srt & 0x7fffu) : (~srt & 0xffffu))));
        *(LAS u32x2*)(size_t)(L_OS + 3072 + (lane >> 4) * 4096 + (lane & 15) * 8) =
            u32x2{__float_as_uint(bfround(bfround(y0) * rsel)), __float_as_uint(bfround(bfround(y1) * rsel))};
      }
    } else {
      NAP(CFIRST + (xc & 3));
      const unsigned ob = WS_OF + ((2 * rb + (w & 1)) * 32 + 4 * hg) * 1024 + lane * 16;
#pragma unroll
      for (int i = 0; i < 4; ++i) ST128((u32x4{~0u, ~0u, ~0u, ~0u}), ob + i * 1024);
      NAP(TFIRST + 9 - CFIRST - (xc & 3));
      DRAIN();
      if (lane == 0 && __hip_atomic_fetch_add((LAS u32*)(size_t)(L_CTL + 24), 1u, __ATOMIC_RELAXED, __HIP_MEMORY_SCOPE_WORKGROUP) == 1)
        ST64((u32x2{tlo, thi}), WS_CL + (rb * 8 + hg) * 128);
      if (w == 10) WORANGE(14, 9, WSLEEP); else WORANGE(23, 9, WSLEEP);
    }
    WAIT_BAR(0);
    BAR();
    return;
  }

  int lane = fresh_lane();
  int c31 = lane & 31, hh = lane >> 5, blk = (lane >> 4) & 1;
  const int pair = w & 1, hi = (w >> 1) & 1;
  GET_TAG();
  if (w < 4) {
    unsigned xa0 = L_X + c31 * 128 + ((hh ^ ((c31 >> 1) & 7)) << 4);
    unsigned xa1 = xa0 ^ 32, xa2 = xa0 ^ 64, xa3 = xa0 ^ 96;
    unsigned wq0 = L_W + (8 * hh + ((lane & 15) >> 2)) * 128 + (((4 * pair + 2 * blk + ((lane & 3) >> 1)) ^ (((lane >> 3) & 1) << 2)) << 4) + 8 * (lane & 1);
    unsigned wa0 = wq0 + hi * 4096, wa1 = wa0 + 65536;
    unsigned o0 = hi == 0 ? L_KV + pair * 8192 + ((c31 & 19) | ((c31 & 4) << 1) | ((c31 & 8) >> 1) | (hh << 5)) * 16
                          : L_KV + pair * 8192 + blk * 4096 + 1024 + (hh * 16 + (lane & 15)) * 16;
    unsigned o1 = o0 + (hi == 0 ? 4096 : 512), oq = L_QX + (4 * pair + hi) * 1024 + lane * 16;
    asm volatile("" : "+v"(xa0), "+v"(xa1), "+v"(xa2), "+v"(xa3), "+v"(wa0), "+v"(wa1), "+v"(wq0), "+v"(o0), "+v"(o1), "+v"(oq));
    const unsigned sy = L_SY + (lane & 3) * 8;
    f32x16 acc0 = {0}, acc1 = {0};
    {
      bf16x8 wf[4], xf[4], xg[4];
      u32x4 p0;
#define WADDR(g, u) ((((2 * (g) + ((u) >> 1)) % 10) >= 8 ? wa1 - 65536 : wa0) + ((2 * (g) + ((u) >> 1)) % 10) * 8192 + ((u) & 1) * 2048)
#define RDF(g, u, xa) do { xf[u] = lds_b128(xa + (g) * 8192); wf[u] = cat8(lds_tr(WADDR(g, u)), lds_tr(WADDR(g, u) + 512)); xg[u] = lds_b128(xa + (g) * 8192 + 4096); SBAR(); } while (0)
#define MA(V, u) do { if (V) acc0 = MFMA32(xf[u], wf[u], acc0); else acc0 = MFMA32(wf[u], xf[u], acc0); SBAR(); } while (0)
#define MB(V, u) do { if (V) acc1 = MFMA32(xg[u], wf[u], acc1); else acc1 = MFMA32(wf[u], xg[u], acc1); SBAR(); } while (0)
#define BLOCK(V, g) do { RDF(g, 0, xa0); if (g) MA(V, 3); RDF(g, 1, xa1); if (g) MB(V, 3); MA(V, 0); RDF(g, 2, xa2); MB(V, 0); MA(V, 1); RDF(g, 3, xa3); MB(V, 1); MA(V, 2); MB(V, 2); } while (0)
#define LANDED(g) for (int spin = 0; spin < (SPIN_MAX << 4); ++spin) { const u32x2 f_ = *(volatile LAS u32x2*)(size_t)(sy + (g) * 32); if (__all(f_[0] == tlo && f_[1] == thi)) break; }
#define KVLOOP(V) do { LANDED(0); BLOCK(V, 0); LANDED(1); BLOCK(V, 1); BAR(); BLOCK(V, 2); BAR(); BLOCK(V, 3); BAR(); BLOCK(V, 4); BAR(); BLOCK(V, 5); \
      BAR(); BLOCK(V, 6); BAR(); BLOCK(V, 7); MA(V, 3); BAR(); MB(V, 3); p0 = pack8(acc0, 0); lds_w128(o0, p0); p0 = pack8(acc0, 1); lds_w128(o1, p0); SBAR(); } while (0)
      if (hi == 0) KVLOOP(false); else KVLOOP(true);
    }
    lds_w128(o0 + 2048, pack8(acc1, 0)); lds_w128(o1 + 2048, pack8(acc1, 1));
    BAR();
    f32x16 qacc = {0};
    bf16x8 wf[2][4], xf[2][4];
    xa0 += hi * 4096; xa1 += hi * 4096; xa2 += hi * 4096; xa3 += hi * 4096;
    unsigned wq1 = wq0 + 65536;
    asm volatile("" : "+v"(wq1));
#define QADDR(r, u) ((QSL(r) >= 65536 ? wq1 - 65536 : wq0) + QSL(r) + (u) * 2048)
#define QR1(r, u, xa) do { xf[(r) & 1][u] = lds_b128(xa + (r) * 8192); \
      wf[(r) & 1][u] = cat8(lds_tr(QADDR(r, u)), lds_tr(QADDR(r, u) + 512)); } while (0)
#define QS(r, u, xa) do { qacc = MFMA32(wf[(r) & 1][u], xf[(r) & 1][u], qacc); SBAR(); if ((r) < 7) QR1((r) + 1, u, xa); SBAR(); } while (0)
#define QG(r) do { QS(r, 0, xa0); QS(r, 1, xa1); QS(r, 2, xa2); QS(r, 3, xa3); } while (0)
    QR1(0, 0, xa0); QR1(0, 1, xa1); QR1(0, 2, xa2); QR1(0, 3, xa3);
    SBAR();
    QG(0); QG(1); QG(2);
    asm volatile("s_barrier" ::: "memory");
    QG(3); QG(4); QG(5); QG(6); QG(7);
    const float qs = 0.25f * 1.4426950408889634f;
    lds_w128(oq, pack8(qacc, 0, qs));
    lds_w128(oq + 2048, pack8(qacc, 1, qs));
  } else {
    const int tok = 16 * (w & 3) + (lane & 15);
    unsigned fa0 = L_WV + (lane & 1) * 1024 + (lane >> 4) * 16;
    unsigned fb0 = L_X + tok * 128 + (((lane >> 4) ^ ((tok >> 1) & 7)) << 4), fb1 = fb0 ^ 64;
    const unsigned gkv = WS_KV + ((8 * hg + 4 * pair + hi) * 32 + rb) * KVB + lane * 16;
    unsigned lkv = L_KV + pair * 8192 + hi * 2048 + lane * 16;
    asm volatile("" : "+v"(fa0), "+v"(fb0), "+v"(fb1), "+v"(lkv));
    f32x4 rgd = {0};
#define RG2(g) do { bf16x8 fa[4], fb[4]; \
      _Pragma("unroll") for (int u = 0; u < 4; ++u) { fb[u] = lds_b128(((u & 1) ? fb1 : fb0) + (2 * (g) + (u >> 1)) * 8192); fa[u] = lds_b128(fa0 + (4 * (g) + u) * 64); } \
      SBAR(); _Pragma("unroll") for (int u = 0; u < 4; ++u) rgd = MFMA16(fa[u], fb[u], rgd); SBAR(); } while (0)
    asm volatile("s_waitcnt vmcnt(6)" ::: "memory");
    *(volatile LAS u32x2*)(size_t)(L_SY + h * 8) = u32x2{tlo, thi};
    DRAIN();
    *(volatile LAS u32x2*)(size_t)(L_SY + 32 + h * 8) = u32x2{tlo, thi};
    __builtin_amdgcn_s_setprio(0);
    if (w == 5) {
      const char* p;
      if (lane < 14) p = (const char*)wg + lane * 131072;
      else if (lane < 28) p = (const char*)wu + (lane - 14) * 131072;
      else if (lane < 42) p = (const char*)wd + (lane - 28) * 131072;
      else if (lane < 46) p = (const char*)wo + (lane - 42) * 131072;
      else if (lane < 52) p = ws + WS_KV + hg * 256 * KVB + (lane - 46) * 98304;
      else if (lane < 53) p = ws + WS_OF + tt * 32768;
      else if (lane < 54) p = ws + WS_FL;
      else if (lane < 55) p = ws + WS_P2;
      else p = (const char*)out + tt * 32768;
      asm volatile("global_load_dword v83, %0, off sc1" :: "v"(p) : "memory", "v83");
    }
    BAR();
    asm volatile("s_barrier" ::: "memory");
    BAR(); RG2(0); BAR(); RG2(1); BAR(); RG2(2); BAR(); RG2(3);
    asm volatile("s_barrier" ::: "memory");
    __builtin_amdgcn_s_setprio(2);
    BAR();
    const u32x4 k0 = *(const LAS u32x4*)(size_t)lkv, v0 = *(const LAS u32x4*)(size_t)(lkv + 1024);
    const u32x4 k1 = *(const LAS u32x4*)(size_t)(lkv + 4096), v1 = *(const LAS u32x4*)(size_t)(lkv + 5120);
    ST128(k0, gkv); ST128(v0, gkv + 1024); ST128(k1, gkv + 64 * KVB); ST128(v1, gkv + 64 * KVB + 1024);
    asm volatile("s_barrier" ::: "memory");
    const unsigned r16 = cvtpk(rgd[0], 0.f) & 0xffffu;
    unsigned key = (((r16 & 0x8000u) ? (~r16 & 0xffffu) : (r16 | 0x8000u)) << 11) | (2047 - (rb * 64 + tok));
    key = max(key, (unsigned)__builtin_amdgcn_update_dpp(0, (int)key, 0x111, 0xf, 0xf, false));
    key = max(key, (unsigned)__builtin_amdgcn_update_dpp(0, (int)key, 0x112, 0xf, 0xf, false));
    key = max(key, (unsigned)__builtin_amdgcn_update_dpp(0, (int)key, 0x114, 0xf, 0xf, false));
    key = max(key, (unsigned)__builtin_amdgcn_update_dpp(0, (int)key, 0x118, 0xf, 0xf, false));
    if (lane < 16) *(LAS float*)(size_t)(L_RG + tok * 4) = bfround(sigmoidf_(bfround(rgd[1])));
    {
      const unsigned fk = (tlo & 0xf8000000u) | __builtin_amdgcn_readlane(key, 15);
      u32x4 fv = {fk, thi, fk, thi};
      unsigned fo = lane < 8 ? WS_FL + ((hg * 4 + 2 * pair + hi) * 32 + rb) * 128 + lane * 16 : 0x7f000000u;
      unsigned ro_ = lane < 32 ? gkv + lane * 112 + (lane >= 16 ? 64 * KVB - 2048 : 0) : 0x7f000000u;
      asm volatile("s_waitcnt vmcnt(0)\n\tbuffer_store_dwordx4 %0, %1, %3, 0 offen sc1\n\tbuffer_load_dword v83, %2, %3, 0 offen sc1\n\ts_nop 0"
                   :: "v"(fv), "v"(fo), "v"(ro_), "s"(rs) : "memory", "v83");
    }
    __builtin_amdgcn_s_setprio(0);
  }
  BAR();

  const int j = w & 3, kh = w >> 2, head = 4 * hg + j;
  lane = fresh_lane(); c31 = lane & 31; hh = lane >> 5;
  const bool onerow = c31 == 16 || c31 == 20;
  unsigned idx = 0xDDDDDDDDu;
  asm volatile("" : "+v"(idx));
  unsigned vk = lane * 16;
  const unsigned vv0 = (hh * 16 + (c31 & 15)) * 16;
  const u64 lm = 0x0000FFFF0000FFFFull;
  const bf16x8 qa = lds_b128(L_QX + (j * 2) * 1024 + vk), qb = lds_b128(L_QX + (j * 2 + 1) * 1024 + vk);
  const unsigned kv0 = WS_KV + (head * 2 + kh) * 32 * KVB, kv1 = kv0 + 32 * KVB;
  unsigned cur = kv0 + rb * KVB;
  auto next = [&]() { cur += KVB; cur = cur == kv1 ? kv0 : cur; return cur; };
  const unsigned lrdy = L_CTL + ((j >> 1) * 2 + kh) * 4;
  u64 ready = 0;
#define NEED(mask) for (int spin = 0; (ready & (mask)) != (mask); ++spin) { if (spin) __builtin_amdgcn_s_sleep(1); \
    ready = 0xffffffff00000000ull | (spin < (SPIN_MAX << 4) ? (unsigned)__builtin_amdgcn_readfirstlane(LDSU(lrdy)) : ~0u); }
#define LDT(s, so_) asm volatile(LDK(s, "%[so]") LDV(s, "%[so]") :: [vk] "v"(vk), [v0] "v"(vv0), [lm] "s"(lm), [rs] "s"(rs), [so] "s"(so_) : "memory", ACLOB)
#define LDK0(so_) asm volatile(LDK(0, "%[so]") :: [vk] "v"(vk), [rs] "s"(rs), [so] "s"(so_) : "memory", ACLOB)
  const unsigned t1 = next(), t2 = next(), t3 = next();
  unsigned s4 = next();
  f32x16 sa, sb;
  {
    const unsigned lk = L_KV + (j * 2 + kh) * 2048;
    const u32x4 z4 = {0, 0, 0, 0};
#define ON1(r) "v_accvgpr_write_b32 a[" STR(r) "], %[on]\n\t"
#define ON8(r) ON1(r) ON1(r+1) ON1(r+2) ON1(r+3) ON1(r+4) ON1(r+5) ON1(r+6) ON1(r+7)
    asm volatile(ON8(48) ON8(56) ON8(64) ON8(72)
                 "ds_read_b128 " KS(0) ", %[la]\n\ts_mov_b64 exec, %[lm]\n\tds_read_b128 " V0(0) ", %[a0] offset:1024\n\t"
                 "ds_read_b128 " V1(0) ", %[a0] offset:1536\n\ts_mov_b64 exec, -1\n\t"
                 "v_mfma_f32_32x32x16_bf16 a[0:15], %[z], %[z], 0\n\t"
                 "v_mfma_f32_32x32x16_bf16 a[16:31], %[z], %[z], 0\n\t"
                 "s_waitcnt lgkmcnt(2)\n\t" QKA(0) QKB(0)
                 "s_waitcnt lgkmcnt(0)\n\ts_barrier\n\ts_nop 3\n\t"
                 EX4(36) EX4(40) EX4(44) SMF(OA, V0(0), 36) EX4(48) EX4(52) SMF(OA, V1(0), 44) EX4(56) EX4(60) SMF(OB, V0(0), 52) EX4(64)
                 : "=&{v[36:51]}"(sa), "=&{v[52:67]}"(sb)
                 : [la] "v"(lk + vk), [a0] "v"(lk + vv0), [lm] "s"(lm), [on] "v"(onerow ? 0x3F803F80u : 0u), [qa] "v"(qa), [qb] "v"(qb), [z] "v"(z4), [idx] "v"(idx)
                 : "memory", ACLOB);
  }
  NEED(0x2ull); LDT(1, t1);
  NEED(0x4ull); LDT(2, t2);
  NEED(0x18ull); LDT(3, t3); LDK0(s4);
  asm volatile("s_waitcnt vmcnt(9)\n\t" QKA(1) "s_nop 15\n\t" : "=&{v[36:51]}"(sa) : [qa] "v"(qa) : "memory", ACLOB);
  __builtin_amdgcn_s_setprio(2);
  unsigned best = 0, s7 = 0;
#pragma unroll 1
  for (int t = 0; t < 32; t += 4) {
    NEED(0xf0ull << t);
    if (t == 24) {
      for (int spin = 0; spin < (SPIN_MAX << 4) && __builtin_amdgcn_readfirstlane(LDSU(L_CTL + 20)) != 4; ++spin) {}
      best = __builtin_amdgcn_readfirstlane(LDSU(L_CTL + 16));
    }
    if (t == 28) cur = 64u << 20;
    if (t) s4 = next();
    const unsigned s5 = next(), s6 = next();
#define PRIO(i) do { if ((i) == PRIO1 % 4 && t == PRIO1 - PRIO1 % 4) __builtin_amdgcn_s_setprio(1); \
      if ((i) == PRIO0 % 4 && t == PRIO0 - PRIO0 % 4 && kh == 0) __builtin_amdgcn_s_setprio(0); } while (0)
    PRIO(0);
    if (t) TILE(0, 3, 1, s7, s4);
    PRIO(1);
    TILE(1, 0, 2, s4, s5);
    PRIO(2);
    s7 = next();
    TILE(2, 1, 3, s5, s6);
    PRIO(3);
    TILE(3, 2, 0, s6, s7);
  }
  float o[9], snd[9];
#define RD(i, r) "v_accvgpr_read_b32 %" STR(i) ", a[" STR(r) "]\n\t"
#define FINAL(F, G) asm volatile("s_nop 1\n\t" SMF(OB, V1(3), 60) \
               RD(0, 0) RD(1, 1) RD(2, 2) RD(3, 3) RD(4, 4) RD(5, 5) RD(6, 6) RD(7, 7) RD(8, 8) "s_waitcnt vmcnt(0)\n\ts_nop 2\n\t" \
               RD(9, 16) RD(10, 17) RD(11, 18) RD(12, 19) RD(13, 20) RD(14, 21) RD(15, 22) RD(16, 23) RD(17, 24) \
               : "=&v"(F[0]), "=&v"(F[1]), "=&v"(F[2]), "=&v"(F[3]), "=&v"(F[4]), "=&v"(F[5]), "=&v"(F[6]), "=&v"(F[7]), "=&v"(F[8]), \
                 "=v"(G[0]), "=v"(G[1]), "=v"(G[2]), "=v"(G[3]), "=v"(G[4]), "=v"(G[5]), "=v"(G[6]), "=v"(G[7]), "=v"(G[8]) \
               : "{v[52:67]}"(sb), [idx] "v"(idx) : "memory", ACLOB)
  if (kh) FINAL(snd, o); else FINAL(o, snd);

  asm volatile("" : "+v"(vk));
  const int ln = vk >> 4;
  const int d31 = ln & 31, dh = ln >> 5;
  const int token = tt * 32 + d31;
  const unsigned xo = (token * D_ + cg * 128 + j * 32 + dh * 16) * 2;
  float gate;
  u32x4 xa, xc;
  {
    const unsigned lo = L_OS + (w ^ 4) * 4096 + vk, li = L_OS + w * 4096 + vk;
    lds_w128(lo, __builtin_bit_cast(u32x4, f32x4{snd[0], snd[1], snd[2], snd[3]})); lds_w128(lo + 1024, __builtin_bit_cast(u32x4, f32x4{snd[4], snd[5], snd[6], snd[7]}));
    *(LAS float*)(size_t)(lo + 2048) = snd[8];
    gate = *(const LAS float*)(size_t)(L_RG + ((hg >> 2) * 32 + (ln & 31)) * 4);
    if (kh == 0) { xa = *(const u32x4*)((const char*)x + xo); xc = *(const u32x4*)((const char*)x + xo + 16); }
    BAR();
    const f32x4 e0 = *(const LAS f32x4*)(size_t)li, e1 = *(const LAS f32x4*)(size_t)(li + 1024);
    const float e2 = *(const LAS float*)(size_t)(li + 2048);
#pragma unroll
    for (int i = 0; i < 4; ++i) { o[i] += e0[i]; o[4 + i] += e1[i]; }
    o[8] += e2;
  }
  const float big = ((fabsf(o[0]) + fabsf(o[1])) + (fabsf(o[2]) + fabsf(o[3]))) + ((fabsf(o[4]) + fabsf(o[5])) + (fabsf(o[6]) + fabsf(o[7])));
  if (__builtin_expect(__any(!(o[8] > 1e-18f && o[8] < 1e18f && big < 1e30f)), 0)) {
    const bf16x8 q = kh ? qb : qa;
    const unsigned kb0 = WS_KV + head * 64 * KVB;
    float m = -3.0e38f;
#pragma unroll 1
    for (int t = 0; t < 64; ++t) {
      const f32x16 s = MFMA32(__builtin_bit_cast(bf16x8, LD128(kb0 + t * KVB + vk)), q, (f32x16{0}));
      m = fmaxf(m, max16(s));
    }
    m = -halves_max(m);
    f32x16 c, oc = {0};
#pragma unroll
    for (int i = 0; i < 16; ++i) c[i] = m;
#pragma unroll 1
    for (int t = 0; t < 64; ++t) {
      const f32x16 s = MFMA32(__builtin_bit_cast(bf16x8, LD128(kb0 + t * KVB + vk)), q, c);
      f32x8 e0, e1;
#pragma unroll
      for (int i = 0; i < 8; ++i) { e0[i] = __builtin_amdgcn_exp2f(s[i]); e1[i] = __builtin_amdgcn_exp2f(s[8 + i]); }
      u32x4 va = LD128(kb0 + t * KVB + 1024 + vv0), vb = LD128(kb0 + t * KVB + 1536 + vv0);
      if (ln & 16) { const unsigned c1 = (ln & 31) == 16 || (ln & 31) == 20 ? 0x3F803F80u : 0u; va = vb = u32x4{c1, c1, c1, c1}; }
      oc = __builtin_amdgcn_smfmac_f32_32x32x32_bf16(__builtin_bit_cast(bf16x8, va), __builtin_bit_cast(bf16x16, e0), oc, (int)idx, 0, 0);
      oc = __builtin_amdgcn_smfmac_f32_32x32x32_bf16(__builtin_bit_cast(bf16x8, vb), __builtin_bit_cast(bf16x16, e1), oc, (int)idx, 0, 0);
    }
#pragma unroll
    for (int i = 0; i < 9; ++i) o[i] = oc[i];
  }
  {
    const float rl = __builtin_amdgcn_rcpf(o[8]);
    const unsigned lim = 0xfffffffeu;
    ST128((u32x4{min(cvtpk(o[0] * rl, o[1] * rl), lim), min(cvtpk(o[2] * rl, o[3] * rl), lim), min(cvtpk(o[4] * rl, o[5] * rl), lim),
                 min(cvtpk(o[6] * rl, o[7] * rl), lim)}), WS_OF + ((2 * rb + kh) * 32 + head) * 1024 + vk);
  }

  SBAR();
  const unsigned abase = (4 * dh + ((ln & 15) >> 2)) * 1024 + (((4 * j + 2 * (ln & 1) + ((ln >> 4) & 1)) ^ (ln & 12)) << 4) + 4 * (ln & 2);
  float tb[16];
  if (kh == 0) {
    asm volatile(OP_PRE :: "v"(abase), "v"(abase + 65536) : "memory", ACLOB);
#pragma unroll
    for (int i = 0; i < 4; ++i) { tb[2 * i] = bflo(xa[i]); tb[2 * i + 1] = bfhi(xa[i]); tb[8 + 2 * i] = bflo(xc[i]); tb[9 + 2 * i] = bfhi(xc[i]); }
    const int sel = 2047 - (int)(best & 2047u);
    if (token == sel) {
#pragma unroll
      for (int i = 0; i < 16; ++i) tb[i] = bfround(tb[i] + *(const LAS float*)(size_t)(L_OS + 3072 + j * 4096 + (dh * 16 + i) * 4));
    }
    const float og = bfround(1.f - gate);
#pragma unroll
    for (int i = 0; i < 16; ++i) { tb[i] *= og; asm volatile("" : "+v"(tb[i])); }
    __builtin_amdgcn_s_sleep(4);
  } else {
    __builtin_amdgcn_s_sleep(12);
  }
  {
    u32x4 v[4];
    for (int spin = 0; spin < SPIN_MAX; ++spin) {
      FRESH();
      unsigned m = 0;
#pragma unroll
      for (int i = 0; i < 4; ++i) v[i] = LD128(WS_OF + (tt * 32 + 4 * w + i) * 1024 + vk);
#pragma unroll
      for (int i = 0; i < 4; ++i) m = max(max(m, max(v[i][0], v[i][1])), max(v[i][2], v[i][3]));
      if (!__any(m == ~0u)) break;
    }
#pragma unroll
    for (int i = 0; i < 4; ++i) lds_w128(L_OS + (4 * w + i) * 1024 + vk, v[i]);
  }
  BAR();
  if (kh == 0) {
    f32x16 a;
    asm volatile(OP_MAIN : "=&{v[68:83]}"(a) : [b] "v"(L_OS + vk), [a0] "v"(abase), [a1] "v"(abase + 65536)
                 : "memory", ACLOB, "v20","v21","v22","v23","v24","v25","v26","v27","v28","v29","v30","v31","v32","v33","v34","v35",
                   "v36","v37","v38","v39","v40","v41","v42","v43","v44","v45","v46","v47","v48","v49","v50","v51");
    float r[16];
    asm volatile("" : "+v"(gate));
#pragma unroll
    for (int i = 0; i < 16; ++i) r[i] = fmaf(gate, a[i], tb[i]);
    const unsigned lw = d31 * 1024 + (((4 * j + 2 * dh) ^ (d31 & 7)) << 4);
    lds_w128(lw, u32x4{cvtpk(r[0], r[1]), cvtpk(r[2], r[3]), cvtpk(r[4], r[5]), cvtpk(r[6], r[7])});
    lds_w128(lw ^ 16, u32x4{cvtpk(r[8], r[9]), cvtpk(r[10], r[11]), cvtpk(r[12], r[13]), cvtpk(r[14], r[15])});
  }
  BAR();
  {
    const int row = 4 * w + (ln >> 4);
    const u32x4 v = *(const LAS u32x4*)(size_t)(row * 1024 + (((ln & 15) ^ (row & 7)) << 4));
    *(u32x4*)((char*)out + ((tt * 32 + row) * D_ + cg * 128 + (ln & 15) * 8) * 2) = v;
    if (b == 0 && w == 0 && ln == 0) ST64((u32x2{tlo, thi}), WS_EP);
  }
}

extern "C" void kernel_launch(void* const* d_in, const int* in_sizes, int n_in, void* d_out, int out_size, void* d_ws, size_t ws_size,
                              hipStream_t stream) {
  (void)hipFuncSetAttribute((const void*)fused, hipFuncAttributeMaxDynamicSharedMemorySize, LDS_BYTES);
  fused<<<256, 768, LDS_BYTES, stream>>>((const u16*)d_in[0], (const u16*)d_in[1], (const u16*)d_in[2], (const u16*)d_in[4],
                                         (const u16*)d_in[5], (const u16*)d_in[6], (const u16*)d_in[7], (const u16*)d_in[8],
                                         (char*)d_ws, (u16*)d_out);
}
```
